```python
import torch

def _softmax(x, axis=-1):
    return torch.softmax(x, dim=axis)

def _gelu(x):
    return torch.nn.functional.gelu(x, approximate='tanh')

def _silu(x):
    return torch.nn.functional.silu(x)

def _sigmoid(x):
    return torch.sigmoid(x)

def _layernorm(x, axis=-1, eps=1e-05):
    m = x.mean(dim=axis, keepdim=True)
    v = x.var(dim=axis, keepdim=True, unbiased=False)
    return (x - m) / torch.sqrt(v + eps)

def _scatter_add(tgt, idx, src, axis=0):
    return tgt.clone().index_add_(axis, idx, src)

def _segment_sum(src, seg, n):
    out = torch.zeros((n,) + src.shape[1:], dtype=src.dtype)
    return out.index_add_(0, seg, src)

def _pad(x, pw):
    flat = tuple((v for pair in reversed(pw) for v in pair))
    return torch.nn.functional.pad(x, flat)
kernel_name = 'adaLN_cond__gqa_block_cached__glu_gated_geglu__prompt'

def setup_inputs():

    _w = lambda a: (lambda t: t.to(torch.bfloat16) if t.is_floating_point() else t)(torch.as_tensor(a))

    def _A_setup():
        B, Sq, D = (1, 2048, 512)
        Hq, Hkv, Sk, Dh = (32, 8, 2048, 128)
        assert Hq % Hkv == 0, (Hq, Hkv)
        return {'x': _w((lambda *s: torch.randn(*s, dtype=torch.bfloat16) * 0.05)(B, Sq, D)), 'k_cache': _w((lambda *s: torch.randn(*s, dtype=torch.bfloat16) * 0.8)(B, Hkv, Sk, Dh)), 'v_cache': _w((lambda *s: torch.randn(*s, dtype=torch.bfloat16) * 3.2)(B, Hkv, Sk, Dh)), 'wq': _w((lambda *s: torch.randn(*s, dtype=torch.bfloat16) * 0.8)(D, Hq * Dh) * D ** (-0.5)), 'wo': _w((lambda *s: torch.randn(*s, dtype=torch.bfloat16) * 3.2)(Hq * Dh, D) * (Hq * Dh) ** (-0.5))}
    _A_in = _A_setup()

    def _B_setup():
        B, S, Dm, Df = (1, 2048, 512, 1792)
        return {'x': _w((lambda *s: torch.randn(*s, dtype=torch.bfloat16) * 0.05)(B, S, Dm)), 'w_gate': _w((lambda *s: torch.randn(*s, dtype=torch.bfloat16) * 3.2)(Dm, Df) * Dm ** (-0.5)), 'w_up': _w((lambda *s: torch.randn(*s, dtype=torch.bfloat16) * 3.2)(Dm, Df) * Dm ** (-0.5)), 'w_down': _w((lambda *s: torch.randn(*s, dtype=torch.bfloat16) * 3.2)(Df, Dm) * Df ** (-0.5))}
    _B_in = _B_setup()
    return {'A_x': _A_in['x'], 'A_k_cache': _A_in['k_cache'], 'A_v_cache': _A_in['v_cache'], 'A_wq': _A_in['wq'], 'A_wo': _A_in['wo'], 'B_x': _B_in['x'], 'B_w_gate': _B_in['w_gate'], 'B_w_up': _B_in['w_up'], 'B_w_down': _B_in['w_down'], 'c': _w((lambda *s: torch.randn(*s, dtype=torch.bfloat16) * 0.4)(1, 512) * 8.0), 'w_mod': _w((lambda *s: torch.randn(*s, dtype=torch.bfloat16) * 0.4)(512, 6 * 512) * 512 ** (-0.5) * 8.0)}

def reference(A_x, A_k_cache, A_v_cache, A_wq, A_wo, B_x, B_w_gate, B_w_up, B_w_down, c, w_mod):

    def _A_ref(x, k_cache, v_cache, wq, wo):
        B, Sq, D = x.shape
        Hq, Dh = (32, 128)
        Hkv = k_cache.shape[1]
        rep = Hq // Hkv
        q = (lambda x, *d: x.permute(*d))((x @ wq).reshape(B, Sq, Hq, Dh), 0, 2, 1, 3)
        k = (lambda x, n, axis: torch.repeat_interleave(x, n, dim=axis))(k_cache, rep, axis=1)
        v = (lambda x, n, axis: torch.repeat_interleave(x, n, dim=axis))(v_cache, rep, axis=1)
        scores = torch.einsum('bhqd,bhkd->bhqk', q, k) * Dh ** (-0.5)
        p = _softmax(scores, axis=-1)
        o = torch.einsum('bhqk,bhkd->bhqd', p, v)
        return (lambda x, *d: x.permute(*d))(o, 0, 2, 1, 3).reshape(B, Sq, Hq * Dh) @ wo

    def _B_ref(x, w_gate, w_up, w_down):
        g = torch.einsum('bsd,df->bsf', x, w_gate)
        u = torch.einsum('bsd,df->bsf', x, w_up)
        h = _gelu(g) * u
        return torch.einsum('bsf,fd->bsd', h, w_down)
    x = A_x
    mod = (_silu(c) @ w_mod).reshape(c.shape[0], 6, 1, -1)
    sa, ca, ga, sb, cb, gb = (mod[:, 0].clone(), mod[:, 1].clone(), mod[:, 2].clone(), mod[:, 3].clone(), mod[:, 4].clone(), mod[:, 5].clone())
    h = _layernorm(x, axis=-1) * (1 + ca) + sa
    x = x + ga * _A_ref(h, A_k_cache, A_v_cache, A_wq, A_wo)
    h = _layernorm(x, axis=-1) * (1 + cb) + sb
    x = x + gb * _B_ref(h, B_w_gate, B_w_up, B_w_down)
    return x
```

```cpp
#include <hip/hip_runtime.h>
#include <hip/hip_bf16.h>
#include <utility>

constexpr int SQ = 2048, DM = 512, HQ = 32, HKV = 8, SK = 2048, DH = 128, DF = 1792;
constexpr size_t WS_MOD  = 0;
constexpr size_t WS_XH   = 16384;
constexpr size_t WS_OF   = WS_XH + (size_t)SQ * DM * 2;
constexpr size_t WS_X1   = WS_OF + (size_t)SQ * HQ * DH * 2;
constexpr size_t WS_ACT  = WS_X1 + (size_t)SQ * DM * 2;
constexpr size_t WS_ST   = WS_ACT + (size_t)SQ * DF * 2;
constexpr size_t WS_WO   = WS_ST + (size_t)SQ * 64;
constexpr size_t WS_WD   = WS_WO + (size_t)HQ * DH * DM * 2;
constexpr size_t WS_WG   = WS_WD + (size_t)DF * DM * 2;
constexpr size_t WS_END  = WS_WG + (size_t)DF * DM * 4;

typedef float    f32x16 __attribute__((ext_vector_type(16)));
typedef float    f32x2  __attribute__((ext_vector_type(2)));
typedef float    f32x4  __attribute__((ext_vector_type(4)));
typedef __bf16   bf16x8 __attribute__((ext_vector_type(8)));
typedef __bf16   bf16x4 __attribute__((ext_vector_type(4)));
typedef __bf16   bf16x2 __attribute__((ext_vector_type(2)));
typedef unsigned u32x4  __attribute__((ext_vector_type(4)));
typedef unsigned u32x2  __attribute__((ext_vector_type(2)));
typedef unsigned short u16;
template <int... Is> using iseq = std::integer_sequence<int, Is...>;
template <int N> using make_iseq = std::make_integer_sequence<int, N>;
#define SRD_WORD3 0x00020000
#define DI __device__ __forceinline__
#define GAP() __builtin_amdgcn_sched_barrier(0)

#define TSDECL
#define TS(i)
#define TSOUT(base)
DI unsigned rfl(unsigned x) { return __builtin_amdgcn_readfirstlane(x); }
DI unsigned cvtpk(float lo, float hi) {
  f32x2 v = {lo, hi}; bf16x2 b = __builtin_convertvector(v, bf16x2); return __builtin_bit_cast(unsigned, b);
}
DI float bflo(unsigned w) { return __uint_as_float(w << 16); }
DI float bfhi(unsigned w) { return __uint_as_float(w & 0xffff0000u); }
DI float rbf(float x) { return bflo(cvtpk(x, 0.f)); }
DI float fma_s(float a, float b, float c) { float r; asm("v_fma_f32 %0, %1, %2, %3" : "=v"(r) : "v"(a), "v"(b), "v"(c)); return r; }
DI void st16(void* p, u32x4 v) { asm volatile("global_store_dwordx4 %0, %1, off sc1\n\ts_nop 1" :: "v"(p), "v"(v) : "memory"); }
DI float wave_sum(float v) {
  #pragma unroll
  for (int o = 32; o > 0; o >>= 1) v += __shfl_xor(v, o);
  return v;
}

__global__ __launch_bounds__(256) void k0_prep(const u16* __restrict__ x, const u16* __restrict__ cvec, const u16* __restrict__ wmod,
                                              float* __restrict__ mod, u16* __restrict__ xh) {
  __shared__ float red[128][17];
  __shared__ float red2[4][16];
  const int tid = threadIdx.x;
  if (blockIdx.x < 192) {
    const int ks = tid >> 1, colb = ((blockIdx.x & 7) * 24 + (blockIdx.x >> 3)) * 16;
    float acc[8] = {0.f, 0.f, 0.f, 0.f, 0.f, 0.f, 0.f, 0.f};
    u32x4 w[4];
    #pragma unroll
    for (int i = 0; i < 4; ++i) w[i] = *(const u32x4*)(wmod + (size_t)(ks * 4 + i) * 3072 + colb + (tid & 1) * 8);
    const u32x2 cv = *(const u32x2*)(cvec + ks * 4);
    #pragma unroll
    for (int i = 0; i < 4; ++i) {
      const float c = (i & 1) ? bfhi(cv[i >> 1]) : bflo(cv[i >> 1]);
      const float s = rbf(c / (1.f + __expf(-c)));
      #pragma unroll
      for (int e = 0; e < 4; ++e) { acc[2 * e] += s * bflo(w[i][e]); acc[2 * e + 1] += s * bfhi(w[i][e]); }
    }
    #pragma unroll
    for (int e = 0; e < 8; ++e) red[ks][(tid & 1) * 8 + e] = acc[e];
    __syncthreads();
    float s = 0.f;
    #pragma unroll
    for (int j = 0; j < 8; ++j) s += red[(tid >> 4) * 8 + j][tid & 15];
    s += __shfl_xor(s, 16); s += __shfl_xor(s, 32);
    if ((tid & 63) < 16) red2[tid >> 6][tid & 15] = s;
    __syncthreads();
    if (tid < 16) {
      const int col = colb + tid;
      float v = rbf((red2[0][tid] + red2[1][tid]) + (red2[2][tid] + red2[3][tid]));
      const int seg = col >> 9;
      if (seg == 1 || seg == 4) v = rbf(1.f + v);
      mod[col] = v;
    }
    return;
  }
  const int lane = tid & 63, row = (blockIdx.x - 192) * 4 + (tid >> 6);
  const u32x4 w = *(const u32x4*)(x + (size_t)row * DM + lane * 8);
  float v[8];
  #pragma unroll
  for (int e = 0; e < 4; ++e) { v[2 * e] = bflo(w[e]); v[2 * e + 1] = bfhi(w[e]); }
  float s = 0.f;
  #pragma unroll
  for (int e = 0; e < 8; ++e) s += v[e];
  const float mean = wave_sum(s) * (1.f / DM);
  float q = 0.f;
  #pragma unroll
  for (int e = 0; e < 8; ++e) { const float d = v[e] - mean; q += d * d; }
  const float var = wave_sum(q) * (1.f / DM);
  const float mb = rbf(mean), sd = rbf(sqrtf(rbf(rbf(var) + 1e-5f)));
  u32x4 o;
  #pragma unroll
  for (int e = 0; e < 4; ++e) o[e] = cvtpk(rbf(v[2 * e] - mb) / sd, rbf(v[2 * e + 1] - mb) / sd);
  *(u32x4*)(xh + ((size_t)((row >> 5) * 32 + (lane >> 1)) * 64 + (lane & 1) * 32 + (row & 31)) * 8) = o;
}

template <int KTOT, int EPI, bool PERMK> struct G64 {
  static constexpr int NCH = KTOT / 4 / 64;
  struct St {
    u32x4 a[3][8], b[3][8];
    f32x16 acc[2][2];
    unsigned aoff0, aoff1, gvo, xvo;
    const char* ap; const char* wp; const char* gp; const char* xp;
    f32x4 eg[2]; u32x4 ex[2];
  };
  template <int I> static DI void issue(St& s) {
    constexpr int set = I % 3;
    const char* ap = s.ap + I * 4096; const char* wp = s.wp + I * 8192;
    #pragma unroll
    for (int k = 0; k < 4; ++k) {
      asm volatile("global_load_dwordx4 %0, %1, %2 offset:%3" : "=&v"(s.b[set][2 * k]) : "v"(s.aoff0), "s"(wp + (k >> 1) * 4096), "i"((k & 1) * 2048) : "memory");
      asm volatile("global_load_dwordx4 %0, %1, %2 offset:%3" : "=&v"(s.b[set][2 * k + 1]) : "v"(s.aoff0), "s"(wp + (k >> 1) * 4096), "i"((k & 1) * 2048 + 1024) : "memory");
      asm volatile("global_load_dwordx4 %0, %1, %2 offset:%3" : "=&v"(s.a[set][k]) : "v"(s.aoff0), "s"(ap), "i"(k * 1024) : "memory");
      asm volatile("global_load_dwordx4 %0, %1, %2 offset:%3" : "=&v"(s.a[set][4 + k]) : "v"(s.aoff1), "s"(ap), "i"(k * 1024) : "memory");
    }
  }
  template <int I> static DI void step(St& s) {
    constexpr int set = I % 3;
    if constexpr (I + 2 < NCH) issue<I + 2>(s);
    if constexpr (I + 3 == NCH) {
      asm volatile("global_load_dwordx4 %0, %1, %2" : "=&v"(s.eg[0]) : "v"(s.gvo), "s"(s.gp) : "memory");
      asm volatile("global_load_dwordx4 %0, %1, %2 offset:16" : "=&v"(s.eg[1]) : "v"(s.gvo), "s"(s.gp) : "memory");
      asm volatile("global_load_dwordx4 %0, %1, %2" : "=&v"(s.ex[0]) : "v"(s.xvo), "s"(s.xp) : "memory");
      asm volatile("global_load_dwordx4 %0, %1, %2" : "=&v"(s.ex[1]) : "v"(s.xvo), "s"(s.xp + 32768) : "memory");
    }
    constexpr int NV = ((I + 2 < NCH) ? 32 : (I + 1 < NCH) ? 16 : 0) + (I + 3 >= NCH ? 4 : 0);
    asm volatile("s_waitcnt vmcnt(%16)" : "+v"(s.a[set][0]), "+v"(s.a[set][1]), "+v"(s.a[set][2]), "+v"(s.a[set][3]),
                 "+v"(s.a[set][4]), "+v"(s.a[set][5]), "+v"(s.a[set][6]), "+v"(s.a[set][7]), "+v"(s.b[set][0]), "+v"(s.b[set][1]), "+v"(s.b[set][2]), "+v"(s.b[set][3]),
                 "+v"(s.b[set][4]), "+v"(s.b[set][5]), "+v"(s.b[set][6]), "+v"(s.b[set][7]) : "i"(NV) : "memory");
    GAP();
    #pragma unroll
    for (int k = 0; k < 4; ++k)
      #pragma unroll
      for (int c = 0; c < 2; ++c) {
        const bf16x8 bf = __builtin_bit_cast(bf16x8, s.b[set][2 * k + c]);
        s.acc[0][c] = __builtin_amdgcn_mfma_f32_32x32x16_bf16(__builtin_bit_cast(bf16x8, s.a[set][k]), bf, s.acc[0][c], 0, 0, 0);
        s.acc[1][c] = __builtin_amdgcn_mfma_f32_32x32x16_bf16(__builtin_bit_cast(bf16x8, s.a[set][4 + k]), bf, s.acc[1][c], 0, 0, 0);
      }
    GAP();
  }
  template <int... Is> static DI void run(St& s, iseq<Is...>) { (step<Is>(s), ...); }
};

template <int KTOT, int EPI, bool PERMK>
__global__ __launch_bounds__(256, 1) void k_gemm64(const u16* __restrict__ Af, const u16* __restrict__ W, const float* __restrict__ mod,
                                                  const u16* __restrict__ xin, u16* __restrict__ x1f, u16* __restrict__ out, float* __restrict__ stats) {
  extern __shared__ __attribute__((aligned(16))) char lds[];
  using G = G64<KTOT, EPI, PERMK>;
  const unsigned tid = threadIdx.x, lane = tid & 63u, wid = rfl(tid >> 6);
  const unsigned bid = blockIdx.x, rt = (bid & 7u) * 4u + ((bid >> 3) & 3u), ct = bid >> 5;
  typename G::St s;
  TSDECL; TS(0);
  const unsigned h = lane >> 5;
  s.wp = (const char*)W + (size_t)(ct * (KTOT / 64) + wid * G::NCH) * 8192u;
  s.aoff0 = lane * 16u; s.aoff1 = lane * 16u + (KTOT / 16) * 1024u;
  s.ap = (const char*)Af + ((size_t)(rt * 2u) * (KTOT / 16) + wid * (KTOT / 64)) * 1024u;
  #pragma unroll
  for (int i = 0; i < 2; ++i)
    #pragma unroll
    for (int c = 0; c < 2; ++c) s.acc[i][c] = f32x16{};
  G::template issue<0>(s); G::template issue<1>(s);
  {
    const unsigned lrow = tid >> 3, lcol = (tid & 7u) * 8u;
    s.gp = (const char*)(mod + (EPI == 0 ? 1024 : 2560) + ct * 64u); s.gvo = lcol * 4u;
    s.xp = EPI == 0 ? (const char*)(xin + (size_t)rt * 64u * DM + ct * 64u) : (const char*)(x1f + (size_t)(rt * 64u + ct * 2u) * 512u);
    s.xvo = EPI == 0 ? (lrow * DM + lcol) * 2u : (((lrow >> 4) * 16u + (lcol >> 5)) * 64u + ((lcol >> 3) & 3u) * 16u + (lrow & 15u)) * 16u;
  }
  TS(1);
  G::run(s, make_iseq<G::NCH>{});
  TS(2);
  float* part = (float*)(lds + wid * 16384u);
  #pragma unroll
  for (int i = 0; i < 2; ++i)
    #pragma unroll
    for (int c = 0; c < 2; ++c)
      #pragma unroll
      for (int r = 0; r < 16; ++r) part[(32 * i + (r & 3) + 8 * (r >> 2) + 4 * h) * 64 + 32 * c + (lane & 31u)] = s.acc[i][c][r];
  __syncthreads();
  asm volatile("s_waitcnt vmcnt(0)" : "+v"(s.eg[0]), "+v"(s.eg[1]), "+v"(s.ex[0]), "+v"(s.ex[1]) :: "memory");
  TS(3);
  #pragma unroll
  for (int it = 0; it < 2; ++it) {
    const unsigned item = tid + 256u * it, row = item >> 3, cg = item & 7u;
    f32x4 v0 = {0.f, 0.f, 0.f, 0.f}, v1 = v0;
    #pragma unroll
    for (int w = 0; w < 4; ++w) {
      const float* pp = (const float*)(lds + w * 16384u) + row * 64 + cg * 8;
      v0 += *(const f32x4*)pp; v1 += *(const f32x4*)(pp + 4);
    }
    const unsigned grow = rt * 64u + row, gcol = ct * 64u + cg * 8u;
    const f32x4 g0 = s.eg[0], g1 = s.eg[1];
    const size_t toff = ((size_t)((grow >> 4) * 16u + (gcol >> 5)) * 64u + ((gcol >> 3) & 3u) * 16u + (grow & 15u)) * 8u;
    const u32x4 xr = s.ex[it];
    u32x4 o;
    o[0] = cvtpk(bflo(xr[0]) + rbf(g0[0] * rbf(v0[0])), bfhi(xr[0]) + rbf(g0[1] * rbf(v0[1])));
    o[1] = cvtpk(bflo(xr[1]) + rbf(g0[2] * rbf(v0[2])), bfhi(xr[1]) + rbf(g0[3] * rbf(v0[3])));
    o[2] = cvtpk(bflo(xr[2]) + rbf(g1[0] * rbf(v1[0])), bfhi(xr[2]) + rbf(g1[1] * rbf(v1[1])));
    o[3] = cvtpk(bflo(xr[3]) + rbf(g1[2] * rbf(v1[2])), bfhi(xr[3]) + rbf(g1[3] * rbf(v1[3])));
    if (EPI == 0) {
      st16(x1f + toff, o);
      float e8[8], sm = 0.f, sq = 0.f;
      #pragma unroll
      for (int e = 0; e < 4; ++e) { e8[2 * e] = bflo(o[e]); e8[2 * e + 1] = bfhi(o[e]); sm += e8[2 * e] + e8[2 * e + 1]; }
      sm += __shfl_xor(sm, 1); sm += __shfl_xor(sm, 2); sm += __shfl_xor(sm, 4);
      const float mt = sm * (1.f / 64.f);
      #pragma unroll
      for (int e = 0; e < 8; ++e) { const float d = e8[e] - mt; sq += d * d; }
      sq += __shfl_xor(sq, 1); sq += __shfl_xor(sq, 2); sq += __shfl_xor(sq, 4);
      if (cg == 0) *(f32x2*)(stats + (grow * 8u + ct) * 2u) = (f32x2){sm, sq};
    } else *(u32x4*)(out + (size_t)grow * DM + gcol) = o;
  }
  TS(4);
  TSOUT((char*)x1f - WS_X1 + ((129u + EPI) << 20));
}

#define K5_GC "v"
#define K5_PD 6
#define K5_PX 3
#define K5_D 5
struct K5 {
  static constexpr int D = K5_D, R = K5_D + 1, PD = K5_PD, PX = K5_PX;
  u32x4 x[2][16];
  f32x16 g[4], u[4];
  static constexpr int O = 7;
  f32x2 t[4];
  u32x4 w[R][2], a[2][4], o;
  f32x4 ms[2][4];
  f32x2 mn[2], rs[2];
  unsigned mvb, vo, wr[2], rd;
  const char* wp; const char* xp;
};
template <int S> DI void k5_wload(K5& s) {
  #pragma unroll
  for (int j = 0; j < 2; ++j)
    asm volatile("global_load_dwordx4 %0, %1, %2 offset:%3" : "=&v"(s.w[S % K5::R][j]) : "v"(s.vo), "s"(s.wp + (S >> 1) * 4096), "i"((S & 1) * 2048 + j * 1024) : "memory");
}
template <int KT> DI void k5_readm(K5& s) {
  #pragma unroll
  for (int j = 0; j < 4; ++j)
    asm volatile("ds_read_b128 %0, %1 offset:%2" : "=&v"(s.ms[KT & 1][j]) : "v"(s.mvb), "i"(KT * 128 + (j & 1) * 16 + (j >> 1) * 2048) : "memory");
}
template <int T, int E> DI void k5_nd(K5& s) {
  constexpr int KT = T >> 1, RB = T & 1;
  const f32x4 (&m)[4] = s.ms[KT & 1];
  const unsigned w = s.x[RB][KT][E];
  f32x2 v = __builtin_elementwise_fma((f32x2){bflo(w), bfhi(w)}, s.rs[RB], s.mn[RB]);
  v = __builtin_elementwise_fma(v, (f32x2){m[2 + (E >> 1)][2 * (E & 1)], m[2 + (E >> 1)][2 * (E & 1) + 1]}, (f32x2){m[E >> 1][2 * (E & 1)], m[E >> 1][2 * (E & 1) + 1]});
  s.o[E] = cvtpk(v[0], v[1]);
  if constexpr (E == 3) asm volatile("ds_write_b128 %0, %1 offset:%2" :: "v"(s.wr[RB]), "v"(s.o), "i"(KT * 2048) : "memory");
}
template <int T, int E> DI void k5_pu(K5& s) { const unsigned w = s.x[T & 1][T >> 1][E]; s.t[E] = (f32x2){bflo(w), bfhi(w)}; }
DI float sfma(float a, float b, float c) { float d; asm("v_fma_f32 %0, %1, %2, %3" : "=v"(d) : "v"(a), "v"(b), "v"(c)); return d; }
template <int T, int E> DI void k5_p1(K5& s) { s.t[E] = (f32x2){sfma(s.t[E][0], s.rs[T & 1][0], s.mn[T & 1][0]), sfma(s.t[E][1], s.rs[T & 1][0], s.mn[T & 1][0])}; }
template <int T, int E> DI void k5_p2(K5& s) {
  const f32x4 (&m)[4] = s.ms[(T >> 1) & 1];
  s.t[E] = (f32x2){sfma(s.t[E][0], m[2 + (E >> 1)][2 * (E & 1)], m[E >> 1][2 * (E & 1)]), sfma(s.t[E][1], m[2 + (E >> 1)][2 * (E & 1) + 1], m[E >> 1][2 * (E & 1) + 1])};
}
template <int T, int E> DI void k5_pc(K5& s) { s.o[E] = cvtpk(s.t[E][0], s.t[E][1]); }
template <int T, int Q> DI void k5_slot(K5& s) {
  if constexpr (Q == 0 && T > K5::O && T <= 32) asm volatile("ds_write_b128 %0, %1 offset:%2" :: "v"(s.wr[(T - 1) & 1]), "v"(s.o), "i"(((T - 1) >> 1) * 2048) : "memory");
  if constexpr (T < 32) {
    if constexpr (Q == 0) { k5_pu<T, 0>(s); }
    if constexpr (Q == 1) { k5_pu<T, 1>(s); k5_p1<T, 0>(s); }
    if constexpr (Q == 2) { k5_pu<T, 2>(s); k5_p1<T, 1>(s); }
    if constexpr (Q == 3) { k5_pu<T, 3>(s); k5_p2<T, 0>(s); }
    if constexpr (Q == 4) { k5_p1<T, 2>(s); k5_p2<T, 1>(s); k5_pc<T, 0>(s); }
    if constexpr (Q == 5) { k5_p1<T, 3>(s); k5_p2<T, 2>(s); k5_pc<T, 1>(s); }
    if constexpr (Q == 6) { k5_p2<T, 3>(s); k5_pc<T, 2>(s); }
    if constexpr (Q == 7) { k5_pc<T, 3>(s); }
  }
}
constexpr int k5_vm(int P) {
  int n = 0, lastx[16] = {}, lastw[32] = {};
  for (int k = 0; k < K5::PX; ++k) { n += 2; lastx[k] = n; }
  for (int k = 0; k < K5::D; ++k) { n += 2; lastw[k] = n; }
  for (int k = K5::PX; k < K5::PD; ++k) { n += 2; lastx[k] = n; }
  for (int q = 0; q < 32 + K5::O; ++q) {
    const int S = q - K5::O;
    if (q == P) {
      int need = q < 32 ? lastx[q >> 1] : 0;
      if (S >= 0 && lastw[S] > need) need = lastw[S];
      return n - need;
    }
    if (S >= 0 && S + K5::D < 32) { n += 2; lastw[S + K5::D] = n; }
    if (q < 32 && (q & 1) == 0 && (q >> 1) + K5::PD < 16) { n += 2; lastx[(q >> 1) + K5::PD] = n; }
  }
  return 0;
}
template <int KT> DI void k5_xload(K5& s) {
  #pragma unroll
  for (int rb = 0; rb < 2; ++rb)
    asm volatile("global_load_dwordx4 %0, %1, %2" : "=&v"(s.x[rb][KT]) : "v"(s.vo), "s"(s.xp + (rb * 16 + KT) * 1024) : "memory");
}
template <int B, int... Is> DI void k5_xloads(K5& s, iseq<Is...>) { (k5_xload<B + Is>(s), ...); }
template <int... Is> DI void k5_wloads(K5& s, iseq<Is...>) { (k5_wload<Is>(s), ...); }
template <int T> DI void k5_tmem(K5& s) {
  if constexpr (T < 32 && (T & 1) == 0) {
    if constexpr ((T >> 1) + K5::PD < 16) k5_xload<(T >> 1) + K5::PD>(s);
    if constexpr ((T >> 1) + 1 < 16) k5_readm<(T >> 1) + 1>(s);
  }
}
template <int T> DI void k5_norm(K5& s) {
  constexpr int KT = T >> 1;
  if constexpr ((T & 1) == 0)
    asm volatile("s_waitcnt vmcnt(%6) lgkmcnt(0)" : "+v"(s.x[0][KT]), "+v"(s.x[1][KT]), "+v"(s.ms[KT & 1][0]), "+v"(s.ms[KT & 1][1]), "+v"(s.ms[KT & 1][2]), "+v"(s.ms[KT & 1][3]) : "i"(k5_vm(T)) : "memory");
  k5_tmem<T>(s);
  k5_nd<T, 0>(s); k5_nd<T, 1>(s); k5_nd<T, 2>(s); k5_nd<T, 3>(s);
  GAP();
}
template <int... Is> DI void k5_norms(K5& s, iseq<Is...>) { (k5_norm<Is>(s), ...); }
template <int S, int RB> DI void k5_aread(K5& s) {
  asm volatile("ds_read_b128 %0, %1 offset:%2" : "=&v"(s.a[S & 1][RB]) : "v"(s.rd + (RB >> 1) * 65536u), "i"((RB & 1) * 32768 + S * 1024) : "memory");
}
template <int S> DI void k5_step(K5& s) {
  constexpr int T = S + K5::O, KT = T < 32 ? T >> 1 : 15;
  asm volatile("s_waitcnt vmcnt(%12) lgkmcnt(0)" : "+v"(s.w[S % K5::R][0]), "+v"(s.w[S % K5::R][1]), "+v"(s.a[S & 1][0]), "+v"(s.a[S & 1][1]), "+v"(s.a[S & 1][2]), "+v"(s.a[S & 1][3]),
               "+v"(s.x[0][KT]), "+v"(s.x[1][KT]), "+v"(s.ms[KT & 1][0]), "+v"(s.ms[KT & 1][1]), "+v"(s.ms[KT & 1][2]), "+v"(s.ms[KT & 1][3]) : "i"(k5_vm(T)) : "memory");
  if constexpr (S > 0 && (S & 3) == 0) asm volatile("s_barrier" ::: "memory");
  const bf16x8 wg = __builtin_bit_cast(bf16x8, s.w[S % K5::R][0]), wu = __builtin_bit_cast(bf16x8, s.w[S % K5::R][1]);
#define K5_MF(acc, C, w, rb) if constexpr (S == 0) asm volatile("v_mfma_f32_32x32x16_bf16 %0, %1, %2, 0" : "=&" C(acc) : "v"(w), "v"(s.a[S & 1][rb])); \
                          else asm volatile("v_mfma_f32_32x32x16_bf16 %0, %1, %2, %0" : "+" C(acc) : "v"(w), "v"(s.a[S & 1][rb]));
#define K5_MM(rb) K5_MF(s.g[rb], K5_GC, wg, rb) GAP(); \
                  if constexpr (S + 1 < 32) k5_aread<S + 1, rb>(s); \
                  if constexpr (rb == 1 && S + K5::D < 32) k5_wload<S + K5::D>(s); \
                  if constexpr (rb == 2) k5_tmem<T>(s); \
                  k5_slot<T, 2 * rb>(s); GAP(); \
                  K5_MF(s.u[rb], "a", wu, rb) GAP(); \
                  k5_slot<T, 2 * rb + 1>(s); GAP();
  K5_MM(0) K5_MM(1) K5_MM(2) K5_MM(3)
#undef K5_MM
#undef K5_MF
}
template <int... Is> DI void k5_run(K5& s, iseq<Is...>) { (k5_step<Is>(s), ...); }

__global__ __launch_bounds__(256, 1) void k5_gateup(const u16* __restrict__ x1f, const u16* __restrict__ wgu,
                                                   const float* __restrict__ mod, const float* __restrict__ stats, u16* __restrict__ actf) {
  extern __shared__ __attribute__((aligned(16))) char lds[];
  const unsigned tid = threadIdx.x, lane = tid & 63u, wid = rfl(tid >> 6);
  const unsigned bid = blockIdx.x, xcd = bid & 7u, idx = bid >> 3;
  const unsigned rb128 = (xcd & 3u) * 4u + (idx & 3u), fb32 = ((xcd >> 2) * 7u + (idx >> 2)) * 4u + wid;
  K5 s;
  TSDECL; TS(0);
  const unsigned ldsb = (unsigned)(size_t)lds;
  const unsigned g = lane >> 4;
  f32x4 st[2][4], mval;
  const char* xp = (const char*)(x1f + (size_t)(rb128 * 8u + wid * 2u) * 16u * 512u);
  s.vo = lane * 16u;
  {
    const char* sp = (const char*)(stats + (size_t)(rb128 * 128u + wid * 32u) * 16u);
    const unsigned so = (lane & 15u) * 64u;
    #pragma unroll
    for (int rb = 0; rb < 2; ++rb)
      #pragma unroll
      for (int j = 0; j < 4; ++j)
        asm volatile("global_load_dwordx4 %0, %1, %2 offset:%3" : "=&v"(st[rb][j]) : "v"(so), "s"(sp), "i"(rb * 1024 + j * 16) : "memory");
    asm volatile("global_load_dwordx4 %0, %1, %2" : "=&v"(mval) : "v"(tid * 16u), "s"(mod + 1536) : "memory");
  }
  s.xp = xp;
  k5_xloads<0>(s, make_iseq<K5::PX>{});
  s.wp = (const char*)wgu + (size_t)fb32 * 65536u;
  k5_wloads(s, make_iseq<K5::D>{});
  k5_xloads<K5::PX>(s, make_iseq<K5::PD - K5::PX>{});
  #pragma unroll
  for (int rb = 0; rb < 2; ++rb) s.wr[rb] = ldsb + wid * 32768u + (g >> 1) * 1024u + ((g & 1u) * 32u + rb * 16u + (lane & 15u)) * 16u;
  s.rd = ldsb + lane * 16u;
  s.mvb = ldsb + 131072u + g * 32u;
  asm volatile("s_waitcnt vmcnt(%9)" : "+v"(mval), "+v"(st[0][0]), "+v"(st[0][1]), "+v"(st[0][2]), "+v"(st[0][3]), "+v"(st[1][0]), "+v"(st[1][1]), "+v"(st[1][2]), "+v"(st[1][3]) : "i"(2 * K5::PD + 2 * K5::D) : "memory");
  TS(1);
  asm volatile("ds_write_b128 %0, %1" :: "v"(ldsb + 131072u + tid * 16u), "v"(mval) : "memory");
  #pragma unroll
  for (int rb = 0; rb < 2; ++rb) {
    float sm = 0.f, sq = 0.f;
    #pragma unroll
    for (int j = 0; j < 4; ++j) { sm += st[rb][j][0] + st[rb][j][2]; sq += st[rb][j][1] + st[rb][j][3]; }
    const float mu = sm * (1.f / DM);
    float dv = 0.f;
    #pragma unroll
    for (int j = 0; j < 4; ++j) { const float d0 = st[rb][j][0] * (1.f / 64.f) - mu, d1 = st[rb][j][2] * (1.f / 64.f) - mu; dv += d0 * d0 + d1 * d1; }
    sq += 64.f * dv;
    const float rstd = 1.f / rbf(sqrtf(rbf(rbf(sq * (1.f / DM)) + 1e-5f))), mean = -rbf(mu) * rstd;
    s.rs[rb] = (f32x2){rstd, rstd}; s.mn[rb] = (f32x2){mean, mean};
  }
  asm volatile("s_waitcnt lgkmcnt(0)\n\ts_barrier" ::: "memory");
  GAP();
  TS(2);
  k5_readm<0>(s);
  k5_norms(s, make_iseq<K5::O>{});
  asm volatile("s_waitcnt lgkmcnt(0)\n\ts_barrier" ::: "memory");
  k5_aread<0, 0>(s); k5_aread<0, 1>(s); k5_aread<0, 2>(s); k5_aread<0, 3>(s);
  GAP();
  TS(3);
  k5_run(s, make_iseq<32>{});
  asm volatile("s_nop 7\n\ts_nop 7\n\ts_nop 3" : "+" K5_GC(s.g[0]), "+" K5_GC(s.g[1]), "+" K5_GC(s.g[2]), "+" K5_GC(s.g[3]), "+a"(s.u[0]), "+a"(s.u[1]), "+a"(s.u[2]), "+a"(s.u[3]));
  TS(4);
  #pragma unroll
  for (int rb = 0; rb < 4; ++rb) {
    u16* op = actf + ((size_t)((rb128 * 4u + rb) * 112u + fb32 * 2u) * 64u + lane) * 8u;
    #pragma unroll
    for (int kk = 0; kk < 2; ++kk) {
      u32x4 o;
      #pragma unroll
      for (int e = 0; e < 4; ++e) {
        const f32x2 gv = {s.g[rb][8 * kk + 2 * e], s.g[rb][8 * kk + 2 * e + 1]}, uv = {s.u[rb][8 * kk + 2 * e], s.u[rb][8 * kk + 2 * e + 1]};
        const f32x2 y2 = gv * __builtin_elementwise_fma(gv * gv, (f32x2){-0.10294323960f, -0.10294323960f}, (f32x2){-2.3022081985f, -2.3022081985f});
        const f32x2 dn = (f32x2){__builtin_amdgcn_exp2f(y2[0]), __builtin_amdgcn_exp2f(y2[1])} + (f32x2){1.f, 1.f};
        const f32x2 av = (gv * (f32x2){__builtin_amdgcn_rcpf(dn[0]), __builtin_amdgcn_rcpf(dn[1])}) * uv;
        o[e] = cvtpk(av[0], av[1]);
      }
      st16(op + kk * 512, o);
    }
  }
  TS(5);
  TSOUT((char*)actf - WS_ACT + (128u << 20));
}

constexpr int NT = SK / 64;
constexpr float THR = 32.0f;
constexpr unsigned KRS = 256u, KTILEB = 64u * KRS;
constexpr size_t SHM_ATTN = 163840;
#define KRING 0x0000u
#define VRING 0x8000u
#define WSTG 0x10000u
#define W4_ACLOB_HI "a128","a129","a130","a131","a132","a133","a134","a135","a136","a137","a138","a139","a140","a141","a142","a143","a144","a145","a146","a147","a148","a149","a150","a151","a152","a153","a154","a155","a156","a157","a158","a159","a160","a161","a162","a163","a164","a165","a166","a167","a168","a169","a170","a171","a172","a173","a174","a175","a176","a177","a178","a179","a180","a181","a182","a183","a184","a185","a186","a187","a188","a189","a190","a191","a192","a193","a194","a195","a196","a197","a198","a199","a200","a201","a202","a203","a204","a205","a206","a207","a208","a209","a210","a211","a212","a213","a214","a215","a216","a217","a218","a219","a220","a221","a222","a223","a224","a225","a226","a227","a228","a229","a230","a231","a232","a233","a234","a235","a236","a237","a238","a239","a240","a241","a242","a243","a244","a245","a246","a247","a248","a249","a250","a251","a252","a253","a254","a255"
#define W4_ACLOB "a0","a1","a2","a3","a4","a5","a6","a7","a8","a9","a10","a11","a12","a13","a14","a15","a16","a17","a18","a19","a20","a21","a22","a23","a24","a25","a26","a27","a28","a29","a30","a31","a32","a33","a34","a35","a36","a37","a38","a39","a40","a41","a42","a43","a44","a45","a46","a47","a48","a49","a50","a51","a52","a53","a54","a55","a56","a57","a58","a59","a60","a61","a62","a63","a64","a65","a66","a67","a68","a69","a70","a71","a72","a73","a74","a75","a76","a77","a78","a79","a80","a81","a82","a83","a84","a85","a86","a87","a88","a89","a90","a91","a92","a93","a94","a95","a96","a97","a98","a99","a100","a101","a102","a103","a104","a105","a106","a107","a108","a109","a110","a111","a112","a113","a114","a115","a116","a117","a118","a119","a120","a121","a122","a123","a124","a125","a126","a127"
#define W4_READ_O(DB, QB, o) do { switch (2 * (DB) + (QB)) { \
    case 0: asm volatile("v_accvgpr_read_b32 %0, a0\n\t" "v_accvgpr_read_b32 %1, a1\n\t" "v_accvgpr_read_b32 %2, a2\n\t" "v_accvgpr_read_b32 %3, a3\n\t" "v_accvgpr_read_b32 %4, a4\n\t" "v_accvgpr_read_b32 %5, a5\n\t" "v_accvgpr_read_b32 %6, a6\n\t" "v_accvgpr_read_b32 %7, a7\n\t" "v_accvgpr_read_b32 %8, a8\n\t" "v_accvgpr_read_b32 %9, a9\n\t" "v_accvgpr_read_b32 %10, a10\n\t" "v_accvgpr_read_b32 %11, a11\n\t" "v_accvgpr_read_b32 %12, a12\n\t" "v_accvgpr_read_b32 %13, a13\n\t" "v_accvgpr_read_b32 %14, a14\n\t" "v_accvgpr_read_b32 %15, a15\n\t" : "=v"((o)[0]), "=v"((o)[1]), "=v"((o)[2]), "=v"((o)[3]), "=v"((o)[4]), "=v"((o)[5]), "=v"((o)[6]), "=v"((o)[7]), "=v"((o)[8]), "=v"((o)[9]), "=v"((o)[10]), "=v"((o)[11]), "=v"((o)[12]), "=v"((o)[13]), "=v"((o)[14]), "=v"((o)[15])); break; \
    case 1: asm volatile("v_accvgpr_read_b32 %0, a16\n\t" "v_accvgpr_read_b32 %1, a17\n\t" "v_accvgpr_read_b32 %2, a18\n\t" "v_accvgpr_read_b32 %3, a19\n\t" "v_accvgpr_read_b32 %4, a20\n\t" "v_accvgpr_read_b32 %5, a21\n\t" "v_accvgpr_read_b32 %6, a22\n\t" "v_accvgpr_read_b32 %7, a23\n\t" "v_accvgpr_read_b32 %8, a24\n\t" "v_accvgpr_read_b32 %9, a25\n\t" "v_accvgpr_read_b32 %10, a26\n\t" "v_accvgpr_read_b32 %11, a27\n\t" "v_accvgpr_read_b32 %12, a28\n\t" "v_accvgpr_read_b32 %13, a29\n\t" "v_accvgpr_read_b32 %14, a30\n\t" "v_accvgpr_read_b32 %15, a31\n\t" : "=v"((o)[0]), "=v"((o)[1]), "=v"((o)[2]), "=v"((o)[3]), "=v"((o)[4]), "=v"((o)[5]), "=v"((o)[6]), "=v"((o)[7]), "=v"((o)[8]), "=v"((o)[9]), "=v"((o)[10]), "=v"((o)[11]), "=v"((o)[12]), "=v"((o)[13]), "=v"((o)[14]), "=v"((o)[15])); break; \
    case 2: asm volatile("v_accvgpr_read_b32 %0, a32\n\t" "v_accvgpr_read_b32 %1, a33\n\t" "v_accvgpr_read_b32 %2, a34\n\t" "v_accvgpr_read_b32 %3, a35\n\t" "v_accvgpr_read_b32 %4, a36\n\t" "v_accvgpr_read_b32 %5, a37\n\t" "v_accvgpr_read_b32 %6, a38\n\t" "v_accvgpr_read_b32 %7, a39\n\t" "v_accvgpr_read_b32 %8, a40\n\t" "v_accvgpr_read_b32 %9, a41\n\t" "v_accvgpr_read_b32 %10, a42\n\t" "v_accvgpr_read_b32 %11, a43\n\t" "v_accvgpr_read_b32 %12, a44\n\t" "v_accvgpr_read_b32 %13, a45\n\t" "v_accvgpr_read_b32 %14, a46\n\t" "v_accvgpr_read_b32 %15, a47\n\t" : "=v"((o)[0]), "=v"((o)[1]), "=v"((o)[2]), "=v"((o)[3]), "=v"((o)[4]), "=v"((o)[5]), "=v"((o)[6]), "=v"((o)[7]), "=v"((o)[8]), "=v"((o)[9]), "=v"((o)[10]), "=v"((o)[11]), "=v"((o)[12]), "=v"((o)[13]), "=v"((o)[14]), "=v"((o)[15])); break; \
    case 3: asm volatile("v_accvgpr_read_b32 %0, a48\n\t" "v_accvgpr_read_b32 %1, a49\n\t" "v_accvgpr_read_b32 %2, a50\n\t" "v_accvgpr_read_b32 %3, a51\n\t" "v_accvgpr_read_b32 %4, a52\n\t" "v_accvgpr_read_b32 %5, a53\n\t" "v_accvgpr_read_b32 %6, a54\n\t" "v_accvgpr_read_b32 %7, a55\n\t" "v_accvgpr_read_b32 %8, a56\n\t" "v_accvgpr_read_b32 %9, a57\n\t" "v_accvgpr_read_b32 %10, a58\n\t" "v_accvgpr_read_b32 %11, a59\n\t" "v_accvgpr_read_b32 %12, a60\n\t" "v_accvgpr_read_b32 %13, a61\n\t" "v_accvgpr_read_b32 %14, a62\n\t" "v_accvgpr_read_b32 %15, a63\n\t" : "=v"((o)[0]), "=v"((o)[1]), "=v"((o)[2]), "=v"((o)[3]), "=v"((o)[4]), "=v"((o)[5]), "=v"((o)[6]), "=v"((o)[7]), "=v"((o)[8]), "=v"((o)[9]), "=v"((o)[10]), "=v"((o)[11]), "=v"((o)[12]), "=v"((o)[13]), "=v"((o)[14]), "=v"((o)[15])); break; \
    case 4: asm volatile("v_accvgpr_read_b32 %0, a64\n\t" "v_accvgpr_read_b32 %1, a65\n\t" "v_accvgpr_read_b32 %2, a66\n\t" "v_accvgpr_read_b32 %3, a67\n\t" "v_accvgpr_read_b32 %4, a68\n\t" "v_accvgpr_read_b32 %5, a69\n\t" "v_accvgpr_read_b32 %6, a70\n\t" "v_accvgpr_read_b32 %7, a71\n\t" "v_accvgpr_read_b32 %8, a72\n\t" "v_accvgpr_read_b32 %9, a73\n\t" "v_accvgpr_read_b32 %10, a74\n\t" "v_accvgpr_read_b32 %11, a75\n\t" "v_accvgpr_read_b32 %12, a76\n\t" "v_accvgpr_read_b32 %13, a77\n\t" "v_accvgpr_read_b32 %14, a78\n\t" "v_accvgpr_read_b32 %15, a79\n\t" : "=v"((o)[0]), "=v"((o)[1]), "=v"((o)[2]), "=v"((o)[3]), "=v"((o)[4]), "=v"((o)[5]), "=v"((o)[6]), "=v"((o)[7]), "=v"((o)[8]), "=v"((o)[9]), "=v"((o)[10]), "=v"((o)[11]), "=v"((o)[12]), "=v"((o)[13]), "=v"((o)[14]), "=v"((o)[15])); break; \
    case 5: asm volatile("v_accvgpr_read_b32 %0, a80\n\t" "v_accvgpr_read_b32 %1, a81\n\t" "v_accvgpr_read_b32 %2, a82\n\t" "v_accvgpr_read_b32 %3, a83\n\t" "v_accvgpr_read_b32 %4, a84\n\t" "v_accvgpr_read_b32 %5, a85\n\t" "v_accvgpr_read_b32 %6, a86\n\t" "v_accvgpr_read_b32 %7, a87\n\t" "v_accvgpr_read_b32 %8, a88\n\t" "v_accvgpr_read_b32 %9, a89\n\t" "v_accvgpr_read_b32 %10, a90\n\t" "v_accvgpr_read_b32 %11, a91\n\t" "v_accvgpr_read_b32 %12, a92\n\t" "v_accvgpr_read_b32 %13, a93\n\t" "v_accvgpr_read_b32 %14, a94\n\t" "v_accvgpr_read_b32 %15, a95\n\t" : "=v"((o)[0]), "=v"((o)[1]), "=v"((o)[2]), "=v"((o)[3]), "=v"((o)[4]), "=v"((o)[5]), "=v"((o)[6]), "=v"((o)[7]), "=v"((o)[8]), "=v"((o)[9]), "=v"((o)[10]), "=v"((o)[11]), "=v"((o)[12]), "=v"((o)[13]), "=v"((o)[14]), "=v"((o)[15])); break; \
    case 6: asm volatile("v_accvgpr_read_b32 %0, a96\n\t" "v_accvgpr_read_b32 %1, a97\n\t" "v_accvgpr_read_b32 %2, a98\n\t" "v_accvgpr_read_b32 %3, a99\n\t" "v_accvgpr_read_b32 %4, a100\n\t" "v_accvgpr_read_b32 %5, a101\n\t" "v_accvgpr_read_b32 %6, a102\n\t" "v_accvgpr_read_b32 %7, a103\n\t" "v_accvgpr_read_b32 %8, a104\n\t" "v_accvgpr_read_b32 %9, a105\n\t" "v_accvgpr_read_b32 %10, a106\n\t" "v_accvgpr_read_b32 %11, a107\n\t" "v_accvgpr_read_b32 %12, a108\n\t" "v_accvgpr_read_b32 %13, a109\n\t" "v_accvgpr_read_b32 %14, a110\n\t" "v_accvgpr_read_b32 %15, a111\n\t" : "=v"((o)[0]), "=v"((o)[1]), "=v"((o)[2]), "=v"((o)[3]), "=v"((o)[4]), "=v"((o)[5]), "=v"((o)[6]), "=v"((o)[7]), "=v"((o)[8]), "=v"((o)[9]), "=v"((o)[10]), "=v"((o)[11]), "=v"((o)[12]), "=v"((o)[13]), "=v"((o)[14]), "=v"((o)[15])); break; \
    case 7: asm volatile("v_accvgpr_read_b32 %0, a112\n\t" "v_accvgpr_read_b32 %1, a113\n\t" "v_accvgpr_read_b32 %2, a114\n\t" "v_accvgpr_read_b32 %3, a115\n\t" "v_accvgpr_read_b32 %4, a116\n\t" "v_accvgpr_read_b32 %5, a117\n\t" "v_accvgpr_read_b32 %6, a118\n\t" "v_accvgpr_read_b32 %7, a119\n\t" "v_accvgpr_read_b32 %8, a120\n\t" "v_accvgpr_read_b32 %9, a121\n\t" "v_accvgpr_read_b32 %10, a122\n\t" "v_accvgpr_read_b32 %11, a123\n\t" "v_accvgpr_read_b32 %12, a124\n\t" "v_accvgpr_read_b32 %13, a125\n\t" "v_accvgpr_read_b32 %14, a126\n\t" "v_accvgpr_read_b32 %15, a127\n\t" : "=v"((o)[0]), "=v"((o)[1]), "=v"((o)[2]), "=v"((o)[3]), "=v"((o)[4]), "=v"((o)[5]), "=v"((o)[6]), "=v"((o)[7]), "=v"((o)[8]), "=v"((o)[9]), "=v"((o)[10]), "=v"((o)[11]), "=v"((o)[12]), "=v"((o)[13]), "=v"((o)[14]), "=v"((o)[15])); break; \
    } } while (0)

DI float max3f(float a, float b, float c) { float r; asm("v_max3_f32 %0, %1, %2, %3" : "=v"(r) : "v"(a), "v"(b), "v"(c)); return r; }
DI float max2f(float a, float b) { float r; asm("v_max_f32_e32 %0, %1, %2" : "=v"(r) : "v"(a), "v"(b)); return r; }
DI float fadd_s(float a, float b) { float r; asm("v_add_f32_e32 %0, %1, %2" : "=v"(r) : "v"(a), "v"(b)); return r; }
DI float fmul_s(float a, float b) { float r; asm("v_mul_f32_e32 %0, %1, %2" : "=v"(r) : "v"(a), "v"(b)); return r; }
DI float fsub_s(float a, float b) { float r; asm("v_sub_f32_e32 %0, %1, %2" : "=v"(r) : "v"(a), "v"(b)); return r; }
DI float w4_aone() { unsigned l; asm volatile("v_mbcnt_lo_u32_b32 %0, -1, 0\n\tv_mbcnt_hi_u32_b32 %0, -1, %0" : "=v"(l)); return l < 32u ? 1.f : 0.f; }
DI void negm_bcast(f32x16& negm, float aone, float neg_mhat) {
  asm volatile("s_nop 1\n\tv_mfma_f32_32x32x2_f32 %0, %1, %2, 0" : "+v"(negm) : "v"(aone), "v"(neg_mhat));
}

struct W4C {
  unsigned vkb[4], vvb, vksrc[2], vvsrc, m0k, m0v, sofk, sofv, ldsb;
  __amdgpu_buffer_rsrc_t srdK, srdV;
  __amdgpu_buffer_rsrc_t srdKn, srdVn, srdQn; unsigned qsofn, qstepn, m0q;
  int T; float THR;
  __amdgpu_buffer_rsrc_t srdR[2]; u16* sdst[2]; u32x2 rd[2];
  unsigned rvo, ra, rl16, rm0[2], rsf[2], rso[2], rnr[2], rn[2], rrel, rinc, rtog, rlast, sso, sinc, stog, rit, radd, m0lim;
  f32x16 S[2][2][2];
  f32x16 NEGM[2];
  bf16x8 Vf[4][4];
  bf16x8 Pk[2][4];
  bf16x4 vlo[4][4], vup[4][4];
  float mhat[2], lsum[2], fct[2]; bool pend;
  float rs0[2], rs1[2], pmx0[2], pmx1[2], rmax[2], delta[2];
  unsigned pm0[8], psf[8];
};

constexpr int AO_(int db, int qb) { return 16 * (2 * db + qb); }
constexpr int AQ_(int qb, int d0) { return 128 + 32 * qb + 4 * d0; }
constexpr int AK_(int kb, int d0) { return 192 + 32 * kb + 4 * d0; }

template <int I> DI void tr_read(W4C& c, unsigned vslot) {
  constexpr int ks = I >> 3, db = (I >> 1) & 3;
  if constexpr ((I & 1) == 0) asm volatile("ds_read_b64_tr_b16 %0, %1 offset:%2" : "=v"(c.vlo[db][ks]) : "v"(c.vvb + vslot), "i"(4096 * ks + 512 * db) : "memory");
  else                        asm volatile("ds_read_b64_tr_b16 %0, %1 offset:%2" : "=v"(c.vup[db][ks]) : "v"(c.vvb + vslot), "i"(4096 * ks + 2048 + 512 * db) : "memory");
}
template <int I> DI void k_read(W4C& c, unsigned slot) {
  constexpr int kb = I >> 3, d0 = I & 7;
  asm volatile("ds_read_b128 a[%c1:%c2], %0 offset:%c3" :: "v"(c.vkb[d0 & 3] + slot), "i"(AK_(kb, d0)), "i"(AK_(kb, d0) + 3), "i"(8192 * kb + 128 * (d0 >> 2)) : "memory", W4_ACLOB_HI);
}
template <int I> DI void q_read(W4C& c, unsigned qoff) {
  constexpr int qb = I >> 3, d0 = I & 7;
  asm volatile("ds_read_b128 a[%c1:%c2], %0 offset:%c3" :: "v"(c.vkb[d0 & 3] + qoff), "i"(AQ_(qb, d0)), "i"(AQ_(qb, d0) + 3), "i"(8192 * qb + 128 * (d0 >> 2)) : "memory", W4_ACLOB_HI);
}
DI bf16x8 q_ldread(unsigned base, const int qb, const int d0) {
  bf16x8 r;
  switch (2 * qb + (d0 >> 2)) {
    case 0: asm volatile("ds_read_b128 %0, %1 offset:0"    : "=v"(r) : "v"(base) : "memory"); break;
    case 1: asm volatile("ds_read_b128 %0, %1 offset:128"  : "=v"(r) : "v"(base) : "memory"); break;
    case 2: asm volatile("ds_read_b128 %0, %1 offset:8192" : "=v"(r) : "v"(base) : "memory"); break;
    default: asm volatile("ds_read_b128 %0, %1 offset:8320" : "=v"(r) : "v"(base) : "memory"); break;
  }
  return r;
}
template <int I> DI void q_write1(const u32x4& w) {
  constexpr int aq = AQ_(I >> 3, I & 7);
  asm volatile("v_accvgpr_write_b32 a[%c4], %0\n\tv_accvgpr_write_b32 a[%c5], %1\n\tv_accvgpr_write_b32 a[%c6], %2\n\tv_accvgpr_write_b32 a[%c7], %3"
               :: "v"(w[0]), "v"(w[1]), "v"(w[2]), "v"(w[3]), "i"(aq), "i"(aq + 1), "i"(aq + 2), "i"(aq + 3) : W4_ACLOB_HI);
}
DI void q_write(const int qb, const int d0, const u32x4& w) {
  switch (8 * qb + d0) {
    case 0: q_write1<0>(w); break;   case 1: q_write1<1>(w); break;   case 2: q_write1<2>(w); break;   case 3: q_write1<3>(w); break;
    case 4: q_write1<4>(w); break;   case 5: q_write1<5>(w); break;   case 6: q_write1<6>(w); break;   case 7: q_write1<7>(w); break;
    case 8: q_write1<8>(w); break;   case 9: q_write1<9>(w); break;   case 10: q_write1<10>(w); break; case 11: q_write1<11>(w); break;
    case 12: q_write1<12>(w); break; case 13: q_write1<13>(w); break; case 14: q_write1<14>(w); break; case 15: q_write1<15>(w); break;
  }
}
template <int X, bool FIRST, int M> DI void qk_mfma(W4C& c) {
  constexpr int d0 = M >> 2, kb = (M >> 1) & 1, qb = M & 1, ak = AK_(kb, d0), aq = AQ_(qb, d0);
  if constexpr (d0 == 0) {
    if constexpr (FIRST) asm volatile("v_mfma_f32_32x32x16_bf16 %0, a[%c1:%c2], a[%c3:%c4], 0" : "=&v"(c.S[X][kb][qb]) : "i"(ak), "i"(ak + 3), "i"(aq), "i"(aq + 3) : W4_ACLOB, W4_ACLOB_HI);
    else                 asm volatile("v_mfma_f32_32x32x16_bf16 %0, a[%c2:%c3], a[%c4:%c5], %1" : "=&v"(c.S[X][kb][qb]) : "v"(c.NEGM[qb]), "i"(ak), "i"(ak + 3), "i"(aq), "i"(aq + 3) : W4_ACLOB, W4_ACLOB_HI);
  } else asm volatile("v_mfma_f32_32x32x16_bf16 %0, a[%c1:%c2], a[%c3:%c4], %0" : "+v"(c.S[X][kb][qb]) : "i"(ak), "i"(ak + 3), "i"(aq), "i"(aq + 3) : W4_ACLOB, W4_ACLOB_HI);
}
template <int M, bool ZERO> DI void pv_mfma(W4C& c) {
  constexpr int ks = M >> 3, db = (M >> 1) & 3, qb = M & 1, ao = AO_(db, qb);
  if constexpr (ZERO && ks == 0) asm volatile("v_mfma_f32_32x32x16_bf16 a[%c2:%c3], %0, %1, 0" :: "v"(c.Vf[db][ks]), "v"(c.Pk[qb][ks]), "i"(ao), "i"(ao + 15) : W4_ACLOB);
  else                         asm volatile("v_mfma_f32_32x32x16_bf16 a[%c2:%c3], %0, %1, a[%c2:%c3]" :: "v"(c.Vf[db][ks]), "v"(c.Pk[qb][ks]), "i"(ao), "i"(ao + 15) : W4_ACLOB);
}
template <int X, int QB, int KS, int P> DI void pack1(W4C& c) {
  u32x4 w = __builtin_bit_cast(u32x4, c.Pk[QB][KS]);
  w[P] = cvtpk(c.S[X][KS >> 1][QB][8 * (KS & 1) + 2 * P], c.S[X][KS >> 1][QB][8 * (KS & 1) + 2 * P + 1]);
  c.Pk[QB][KS] = __builtin_bit_cast(bf16x8, w);
}
template <int X, int J> DI void rowsum1(W4C& c) {
  constexpr int qb = J >> 4, ks = (J >> 2) & 3, p = J & 3;
  c.rs0[qb] = fadd_s(c.rs0[qb], c.S[X][ks >> 1][qb][8 * (ks & 1) + 2 * p]); c.rs1[qb] = fadd_s(c.rs1[qb], c.S[X][ks >> 1][qb][8 * (ks & 1) + 2 * p + 1]);
}

template <int X, bool FIRST, int M, bool NEXTK = false, bool NEXTV = false> DI void gapA(W4C& c, unsigned vslot, const int t) {
  constexpr int Xp = X ^ 1;
  qk_mfma<X, FIRST, M>(c);
  if constexpr (M >= 16 && M <= 31) {
    const int tk = NEXTK ? t + 2 - c.T : t + 2, tv = NEXTV ? t + 1 - c.T : t + 1;
    constexpr int q = (M - 16) >> 1;
    if constexpr (((M - 16) & 1) == 0) {
      if constexpr (q < 4) c.pm0[q] = c.m0k + (unsigned)((t + 2) & 1) * 0x4000u + 1024u * q; else c.pm0[q] = c.m0v + (unsigned)((t + 1) & 1) * 0x4000u + 1024u * (q - 4);
      asm volatile("" : "+s"(c.pm0[q]));
    } else {
      if constexpr (q < 4) c.psf[q] = KTILEB * (unsigned)tk + c.sofk + KRS * 4u * q; else c.psf[q] = KTILEB * (unsigned)tv + c.sofv + KRS * 8u * ((q - 4) >> 1) + 128u * ((q - 4) & 1);
      asm volatile("" : "+s"(c.psf[q]));
    }
  }
  if constexpr (M == 31) asm volatile("s_nop 4" : "+s"(c.pm0[0]), "+s"(c.pm0[1]), "+s"(c.pm0[2]), "+s"(c.pm0[3]), "+s"(c.pm0[4]), "+s"(c.pm0[5]), "+s"(c.pm0[6]), "+s"(c.pm0[7]),
                                                  "+s"(c.psf[0]), "+s"(c.psf[1]), "+s"(c.psf[2]), "+s"(c.psf[3]), "+s"(c.psf[4]), "+s"(c.psf[5]), "+s"(c.psf[6]), "+s"(c.psf[7]));
  if constexpr (M == 21 || M == 22) asm volatile("ds_read_b64_tr_b16 %0, %1 offset:%2" : "=&v"(c.rd[M - 21]) : "v"(c.ra), "i"(X * 9216 + (M - 21) * 1024) : "memory");
  if constexpr (X == 1 && M == 28) asm volatile("v_xor_b32_e32 %0, 64, %0" : "+v"(c.ra));
  if constexpr (X == 1 && M == 29) asm volatile("v_add_u32_e32 %0, %1, %0" : "+v"(c.ra) : "s"(c.radd));
  if constexpr (!FIRST && M == 8) asm volatile("" :: "v"(c.NEGM[0]), "v"(c.NEGM[1]));
  if constexpr (!FIRST) {
    constexpr int base = M + (M >= 4) + (M >= 8) + (M >= 12) + (M >= 16) + (M >= 20) + (M >= 24);
    if constexpr (M <= 25) tr_read<base>(c, vslot);
    if constexpr (M == 3 || M == 7 || M == 11 || M == 15 || M == 19 || M == 23) tr_read<base + 1>(c, vslot);
    if constexpr (M < 16) {
      constexpr int e0 = 2 * M, e1 = 2 * M + 1;
      c.S[Xp][1][e0 >> 4][e0 & 15] = __builtin_amdgcn_exp2f(c.S[Xp][1][e0 >> 4][e0 & 15]);
      c.S[Xp][1][e1 >> 4][e1 & 15] = __builtin_amdgcn_exp2f(c.S[Xp][1][e1 >> 4][e1 & 15]);
      pack1<Xp, (M >> 3), ((M >> 2) & 1), (M & 3)>(c);
    } else {
      constexpr int cc = M - 16;
      pack1<Xp, (cc >> 3), 2 + ((cc >> 2) & 1), (cc & 3)>(c);
      rowsum1<Xp, 16 * (cc >> 3) + 8 + 4 * ((cc >> 2) & 1) + (cc & 3)>(c);
    }
  }
  GAP();
}
template <int X, bool FIRST, bool NEXTK, bool NEXTV, int VM, int... Ms> DI void phaseA_(W4C& c, const int t, iseq<Ms...>) {
  const unsigned vslot = (unsigned)((t - 1) & 1) * 0x4000u;
  (gapA<X, FIRST, Ms, NEXTK, NEXTV>(c, vslot, t), ...);
  if constexpr (!FIRST) {
    #pragma unroll
    for (int qb = 0; qb < 2; ++qb) {
      const float rs = fadd_s(c.rs0[qb], c.rs1[qb]);
      auto pr = __builtin_amdgcn_permlane32_swap(__float_as_uint(rs), __float_as_uint(rs), false, false);
      c.lsum[qb] = fadd_s(c.lsum[qb], fadd_s(__uint_as_float(pr[0]), __uint_as_float(pr[1])));
    }
  }
#define W4_VLO "+v"(c.vlo[0][0]), "+v"(c.vlo[0][1]), "+v"(c.vlo[0][2]), "+v"(c.vlo[0][3]), "+v"(c.vlo[1][0]), "+v"(c.vlo[1][1]), "+v"(c.vlo[1][2]), "+v"(c.vlo[1][3]), \
               "+v"(c.vlo[2][0]), "+v"(c.vlo[2][1]), "+v"(c.vlo[2][2]), "+v"(c.vlo[2][3]), "+v"(c.vlo[3][0]), "+v"(c.vlo[3][1]), "+v"(c.vlo[3][2]), "+v"(c.vlo[3][3])
#define W4_VUP "+v"(c.vup[0][0]), "+v"(c.vup[0][1]), "+v"(c.vup[0][2]), "+v"(c.vup[0][3]), "+v"(c.vup[1][0]), "+v"(c.vup[1][1]), "+v"(c.vup[1][2]), "+v"(c.vup[1][3]), \
               "+v"(c.vup[2][0]), "+v"(c.vup[2][1]), "+v"(c.vup[2][2]), "+v"(c.vup[2][3]), "+v"(c.vup[3][0]), "+v"(c.vup[3][1]), "+v"(c.vup[3][2]), "+v"(c.vup[3][3])
  if constexpr (FIRST) asm volatile("s_waitcnt vmcnt(0) lgkmcnt(0)\n\ts_barrier" ::: "memory");
  else { asm volatile("s_waitcnt vmcnt(%16) lgkmcnt(0)" : W4_VLO : "i"(VM) : "memory"); asm volatile("s_barrier" : W4_VUP :: "memory"); }
  asm volatile("" : "+v"(c.rd[0]), "+v"(c.rd[1]) :: "memory");
  GAP();
  if constexpr (!FIRST) {
    #pragma unroll
    for (int ks = 0; ks < 4; ++ks)
      #pragma unroll
      for (int db = 0; db < 4; ++db) c.Vf[db][ks] = __builtin_shufflevector(c.vlo[db][ks], c.vup[db][ks], 0, 1, 2, 3, 4, 5, 6, 7);
  }
}
template <int X, bool FIRST, int VM = 2> DI void phaseA(W4C& c, const int t) { phaseA_<X, FIRST, false, false, VM>(c, t, make_iseq<32>{}); }

#define RLG 12
#define RSG 16
template <int X, bool PEEL, bool ZERO1, int DK, int DV, bool KR, int M> DI void gapB(W4C& c, const unsigned kslot_rd) {
  if constexpr (!PEEL) pv_mfma<M, ZERO1>(c);
  if constexpr (M < 4 && DK == 1)             asm volatile("s_mov_b32 m0, %0\n\ts_nop 0\n\tbuffer_load_dwordx4 %1, %2, %3 offen lds" :: "s"(c.pm0[M]), "v"(c.vksrc[M & 1]), "s"(c.srdK),  "s"(c.psf[M]) : "m0", "memory");
  else if constexpr (M < 4 && DK == 2)        asm volatile("s_mov_b32 m0, %0\n\ts_nop 0\n\tbuffer_load_dwordx4 %1, %2, %3 offen lds" :: "s"(c.pm0[M]), "v"(c.vksrc[M & 1]), "s"(c.srdKn), "s"(c.psf[M]) : "m0", "memory");
  else if constexpr (M >= 4 && M < 8 && DV == 1) asm volatile("s_mov_b32 m0, %0\n\ts_nop 0\n\tbuffer_load_dwordx4 %1, %2, %3 offen lds" :: "s"(c.pm0[M]), "v"(c.vvsrc), "s"(c.srdV),  "s"(c.psf[M]) : "m0", "memory");
  else if constexpr (M >= 4 && M < 8 && DV == 2) asm volatile("s_mov_b32 m0, %0\n\ts_nop 0\n\tbuffer_load_dwordx4 %1, %2, %3 offen lds" :: "s"(c.pm0[M]), "v"(c.vvsrc), "s"(c.srdVn), "s"(c.psf[M]) : "m0", "memory");
  if constexpr (DK == 1 && DV == 1) {
    if constexpr (M == RLG) asm volatile("s_mov_b32 m0, %0\n\ts_nop 0\n\tbuffer_load_dwordx4 %1, %2, %3 offen lds" :: "s"(c.rm0[X]), "v"(c.rvo), "s"(c.srdR[X]), "s"(c.rsf[X]) : "m0", "memory");
    if constexpr (M == RSG) asm volatile("buffer_store_dwordx4 %0, %1, %2, %3 offen sc1\n\ts_nop 1" :: "v"((u32x4){c.rd[0][0], c.rd[0][1], c.rd[1][0], c.rd[1][1]}), "v"(c.rl16),
                                       "s"(__builtin_amdgcn_make_buffer_rsrc((void*)c.sdst[X], (short)0, c.rnr[X], SRD_WORD3)), "s"(c.sso) : "memory");
    if constexpr (X == 1) {
      if constexpr (M == 21) { c.rm0[0] = min(c.rm0[0] + 1024u, c.m0lim); asm volatile("" : "+s"(c.rm0[0])); }
      if constexpr (M == 22) { c.rm0[1] = c.rm0[0] + 9216u; asm volatile("" : "+s"(c.rm0[1])); }
      if constexpr (M == 23) { c.rrel = min(c.rrel + c.rinc, c.rlast); asm volatile("" : "+s"(c.rrel)); }
      if constexpr (M == 24) { c.rinc ^= c.rtog; asm volatile("" : "+s"(c.rinc)); }
      if constexpr (M == 25) { c.rsf[0] = c.rso[0] + c.rrel; asm volatile("" : "+s"(c.rsf[0])); }
      if constexpr (M == 27) { c.rsf[1] = c.rso[1] + c.rrel; asm volatile("" : "+s"(c.rsf[1])); }
      if constexpr (M == 28) { c.sso += c.sinc; asm volatile("" : "+s"(c.sso)); }
      if constexpr (M == 29) { c.sinc ^= c.stog; asm volatile("" : "+s"(c.sinc)); }
      if constexpr (M == 29) { c.rit += 1u; asm volatile("" : "+s"(c.rit)); }
      if constexpr (M == 30) { c.rnr[0] = c.rit < 8u ? c.rn[0] : 0u; c.rnr[1] = c.rit < 8u ? c.rn[1] : 0u; asm volatile("" : "+s"(c.rnr[0]), "+s"(c.rnr[1])); }
      if constexpr (M == 31) { c.radd ^= 2048u; asm volatile("" : "+s"(c.radd)); }
    }
  }
  if constexpr (M >= 1 && M <= 16 && KR) k_read<M - 1>(c, kslot_rd);
  if constexpr (M >= 8 && M < 16) {
    constexpr int i = M - 8, q = i >> 2, r = 4 * (i & 3);
    if constexpr ((i & 3) == 0) { c.pmx0[q] = max3f(c.S[X][0][q][0], c.S[X][0][q][1], c.S[X][1][q][0]); c.pmx1[q] = max3f(c.S[X][0][q][2], c.S[X][0][q][3], c.S[X][1][q][1]);
                                  c.pmx0[q] = max3f(c.pmx0[q], c.S[X][1][q][2], c.S[X][1][q][3]); }
    else                        { c.pmx0[q] = max3f(c.pmx0[q], c.S[X][0][q][r], c.S[X][0][q][r + 1]); c.pmx1[q] = max3f(c.pmx1[q], c.S[X][0][q][r + 2], c.S[X][0][q][r + 3]);
                                  c.pmx0[q] = max3f(c.pmx0[q], c.S[X][1][q][r], c.S[X][1][q][r + 1]); c.pmx1[q] = max3f(c.pmx1[q], c.S[X][1][q][r + 2], c.S[X][1][q][r + 3]); }
  }
  if constexpr (M == 16 || M == 17) {
    constexpr int q = M - 16; const float mm = max2f(c.pmx0[q], c.pmx1[q]);
    auto pr = __builtin_amdgcn_permlane32_swap(__float_as_uint(mm), __float_as_uint(mm), false, false);
    c.rmax[q] = max2f(__uint_as_float(pr[0]), __uint_as_float(pr[1]));
  }
  if constexpr (M == 18) {
    if constexpr (PEEL) {
      #pragma unroll
      for (int q = 0; q < 2; ++q) { c.mhat[q] = c.rmax[q]; c.lsum[q] = 0.f; c.fct[q] = 1.f;
        #pragma unroll
        for (int kb = 0; kb < 2; ++kb) { _Pragma("unroll") for (int e = 0; e < 16; ++e) c.S[X][kb][q][e] = fsub_s(c.S[X][kb][q][e], c.rmax[q]); }
        negm_bcast(c.NEGM[q], w4_aone(), -c.mhat[q]);
      }
    } else {
      const bool grew = (c.rmax[0] > c.THR) | (c.rmax[1] > c.THR);
      if (__builtin_expect(__any((int)grew), 0)) {
        #pragma unroll
        for (int q = 0; q < 2; ++q) { const float dl = max2f(c.rmax[q], 0.f); c.delta[q] = dl; c.mhat[q] = fadd_s(c.mhat[q], dl); c.fct[q] = __builtin_amdgcn_exp2f(-dl);
          #pragma unroll
          for (int kb = 0; kb < 2; ++kb) { _Pragma("unroll") for (int e = 0; e < 16; ++e) c.S[X][kb][q][e] = fsub_s(c.S[X][kb][q][e], dl); }
          negm_bcast(c.NEGM[q], w4_aone(), -c.mhat[q]);
        }
        c.pend = true;
      }
    }
  }
  if constexpr (M == 18) { c.rs0[0] = c.rs0[1] = c.rs1[0] = c.rs1[1] = 0.f; }
  if constexpr (M >= 19) {
    constexpr int g = M - 19, e_lo = (32 * g) / 13, e_hi = (32 * (g + 1)) / 13;
    {
      constexpr int p_lo = g ? (32 * (g - 1)) / 13 : 0, p_hi = g ? (32 * g) / 13 : 0;
      if constexpr (p_lo + 0 < p_hi) { constexpr int e = p_lo + 0; if constexpr (e & 1) c.rs1[e >> 4] = fadd_s(c.rs1[e >> 4], c.S[X][0][e >> 4][e & 15]); else c.rs0[e >> 4] = fadd_s(c.rs0[e >> 4], c.S[X][0][e >> 4][e & 15]); }
      if constexpr (p_lo + 1 < p_hi) { constexpr int e = p_lo + 1; if constexpr (e & 1) c.rs1[e >> 4] = fadd_s(c.rs1[e >> 4], c.S[X][0][e >> 4][e & 15]); else c.rs0[e >> 4] = fadd_s(c.rs0[e >> 4], c.S[X][0][e >> 4][e & 15]); }
      if constexpr (p_lo + 2 < p_hi) { constexpr int e = p_lo + 2; if constexpr (e & 1) c.rs1[e >> 4] = fadd_s(c.rs1[e >> 4], c.S[X][0][e >> 4][e & 15]); else c.rs0[e >> 4] = fadd_s(c.rs0[e >> 4], c.S[X][0][e >> 4][e & 15]); }
    }
    if constexpr (e_lo + 0 < e_hi) c.S[X][0][(e_lo + 0) >> 4][(e_lo + 0) & 15] = __builtin_amdgcn_exp2f(c.S[X][0][(e_lo + 0) >> 4][(e_lo + 0) & 15]);
    if constexpr (e_lo + 1 < e_hi) c.S[X][0][(e_lo + 1) >> 4][(e_lo + 1) & 15] = __builtin_amdgcn_exp2f(c.S[X][0][(e_lo + 1) >> 4][(e_lo + 1) & 15]);
    if constexpr (e_lo + 2 < e_hi) c.S[X][0][(e_lo + 2) >> 4][(e_lo + 2) & 15] = __builtin_amdgcn_exp2f(c.S[X][0][(e_lo + 2) >> 4][(e_lo + 2) & 15]);
  }
  if constexpr (M == 31) asm volatile("" :: "v"(c.S[X][0][0]), "v"(c.S[X][0][1]), "v"(c.NEGM[0]), "v"(c.NEGM[1]), "v"(c.rs0[0]), "v"(c.rs0[1]), "v"(c.rs1[0]), "v"(c.rs1[1]));
  GAP();
}
template <int X, bool PEEL, bool ZERO1, int DK, int DV, bool KR, int... Ms> DI void phaseB_(W4C& c, const int t, iseq<Ms...>) {
  const unsigned kslot_rd = (unsigned)((t + 1) & 1) * 0x4000u;
  if constexpr (!PEEL) asm volatile("s_nop 1" ::: "memory");
  GAP();
  (gapB<X, PEEL, ZERO1, DK, DV, KR, Ms>(c, kslot_rd), ...);
  {
    #pragma unroll
    for (int e = (32 * 12) / 13; e < 32; ++e) { if (e & 1) c.rs1[e >> 4] = fadd_s(c.rs1[e >> 4], c.S[X][0][e >> 4][e & 15]); else c.rs0[e >> 4] = fadd_s(c.rs0[e >> 4], c.S[X][0][e >> 4][e & 15]); }
  }
  if constexpr (!PEEL) {
    if (__builtin_expect(c.pend, 0)) {
      float t0, t1;
      asm volatile("s_nop 7\n\ts_nop 7\n\t"
        "v_accvgpr_read_b32 %0, a0\n\tv_mul_f32 %0, %2, %0\n\tv_accvgpr_write_b32 a0, %0\n\t"
        "v_accvgpr_read_b32 %1, a1\n\tv_mul_f32 %1, %2, %1\n\tv_accvgpr_write_b32 a1, %1\n\t"
        "v_accvgpr_read_b32 %0, a2\n\tv_mul_f32 %0, %2, %0\n\tv_accvgpr_write_b32 a2, %0\n\t"
        "v_accvgpr_read_b32 %1, a3\n\tv_mul_f32 %1, %2, %1\n\tv_accvgpr_write_b32 a3, %1\n\t"
        "v_accvgpr_read_b32 %0, a4\n\tv_mul_f32 %0, %2, %0\n\tv_accvgpr_write_b32 a4, %0\n\t"
        "v_accvgpr_read_b32 %1, a5\n\tv_mul_f32 %1, %2, %1\n\tv_accvgpr_write_b32 a5, %1\n\t"
        "v_accvgpr_read_b32 %0, a6\n\tv_mul_f32 %0, %2, %0\n\tv_accvgpr_write_b32 a6, %0\n\t"
        "v_accvgpr_read_b32 %1, a7\n\tv_mul_f32 %1, %2, %1\n\tv_accvgpr_write_b32 a7, %1\n\t"
        "v_accvgpr_read_b32 %0, a8\n\tv_mul_f32 %0, %2, %0\n\tv_accvgpr_write_b32 a8, %0\n\t"
        "v_accvgpr_read_b32 %1, a9\n\tv_mul_f32 %1, %2, %1\n\tv_accvgpr_write_b32 a9, %1\n\t"
        "v_accvgpr_read_b32 %0, a10\n\tv_mul_f32 %0, %2, %0\n\tv_accvgpr_write_b32 a10, %0\n\t"
        "v_accvgpr_read_b32 %1, a11\n\tv_mul_f32 %1, %2, %1\n\tv_accvgpr_write_b32 a11, %1\n\t"
        "v_accvgpr_read_b32 %0, a12\n\tv_mul_f32 %0, %2, %0\n\tv_accvgpr_write_b32 a12, %0\n\t"
        "v_accvgpr_read_b32 %1, a13\n\tv_mul_f32 %1, %2, %1\n\tv_accvgpr_write_b32 a13, %1\n\t"
        "v_accvgpr_read_b32 %0, a14\n\tv_mul_f32 %0, %2, %0\n\tv_accvgpr_write_b32 a14, %0\n\t"
        "v_accvgpr_read_b32 %1, a15\n\tv_mul_f32 %1, %2, %1\n\tv_accvgpr_write_b32 a15, %1\n\t"
        "v_accvgpr_read_b32 %0, a16\n\tv_mul_f32 %0, %3, %0\n\tv_accvgpr_write_b32 a16, %0\n\t"
        "v_accvgpr_read_b32 %1, a17\n\tv_mul_f32 %1, %3, %1\n\tv_accvgpr_write_b32 a17, %1\n\t"
        "v_accvgpr_read_b32 %0, a18\n\tv_mul_f32 %0, %3, %0\n\tv_accvgpr_write_b32 a18, %0\n\t"
        "v_accvgpr_read_b32 %1, a19\n\tv_mul_f32 %1, %3, %1\n\tv_accvgpr_write_b32 a19, %1\n\t"
        "v_accvgpr_read_b32 %0, a20\n\tv_mul_f32 %0, %3, %0\n\tv_accvgpr_write_b32 a20, %0\n\t"
        "v_accvgpr_read_b32 %1, a21\n\tv_mul_f32 %1, %3, %1\n\tv_accvgpr_write_b32 a21, %1\n\t"
        "v_accvgpr_read_b32 %0, a22\n\tv_mul_f32 %0, %3, %0\n\tv_accvgpr_write_b32 a22, %0\n\t"
        "v_accvgpr_read_b32 %1, a23\n\tv_mul_f32 %1, %3, %1\n\tv_accvgpr_write_b32 a23, %1\n\t"
        "v_accvgpr_read_b32 %0, a24\n\tv_mul_f32 %0, %3, %0\n\tv_accvgpr_write_b32 a24, %0\n\t"
        "v_accvgpr_read_b32 %1, a25\n\tv_mul_f32 %1, %3, %1\n\tv_accvgpr_write_b32 a25, %1\n\t"
        "v_accvgpr_read_b32 %0, a26\n\tv_mul_f32 %0, %3, %0\n\tv_accvgpr_write_b32 a26, %0\n\t"
        "v_accvgpr_read_b32 %1, a27\n\tv_mul_f32 %1, %3, %1\n\tv_accvgpr_write_b32 a27, %1\n\t"
        "v_accvgpr_read_b32 %0, a28\n\tv_mul_f32 %0, %3, %0\n\tv_accvgpr_write_b32 a28, %0\n\t"
        "v_accvgpr_read_b32 %1, a29\n\tv_mul_f32 %1, %3, %1\n\tv_accvgpr_write_b32 a29, %1\n\t"
        "v_accvgpr_read_b32 %0, a30\n\tv_mul_f32 %0, %3, %0\n\tv_accvgpr_write_b32 a30, %0\n\t"
        "v_accvgpr_read_b32 %1, a31\n\tv_mul_f32 %1, %3, %1\n\tv_accvgpr_write_b32 a31, %1\n\t"
        "v_accvgpr_read_b32 %0, a32\n\tv_mul_f32 %0, %2, %0\n\tv_accvgpr_write_b32 a32, %0\n\t"
        "v_accvgpr_read_b32 %1, a33\n\tv_mul_f32 %1, %2, %1\n\tv_accvgpr_write_b32 a33, %1\n\t"
        "v_accvgpr_read_b32 %0, a34\n\tv_mul_f32 %0, %2, %0\n\tv_accvgpr_write_b32 a34, %0\n\t"
        "v_accvgpr_read_b32 %1, a35\n\tv_mul_f32 %1, %2, %1\n\tv_accvgpr_write_b32 a35, %1\n\t"
        "v_accvgpr_read_b32 %0, a36\n\tv_mul_f32 %0, %2, %0\n\tv_accvgpr_write_b32 a36, %0\n\t"
        "v_accvgpr_read_b32 %1, a37\n\tv_mul_f32 %1, %2, %1\n\tv_accvgpr_write_b32 a37, %1\n\t"
        "v_accvgpr_read_b32 %0, a38\n\tv_mul_f32 %0, %2, %0\n\tv_accvgpr_write_b32 a38, %0\n\t"
        "v_accvgpr_read_b32 %1, a39\n\tv_mul_f32 %1, %2, %1\n\tv_accvgpr_write_b32 a39, %1\n\t"
        "v_accvgpr_read_b32 %0, a40\n\tv_mul_f32 %0, %2, %0\n\tv_accvgpr_write_b32 a40, %0\n\t"
        "v_accvgpr_read_b32 %1, a41\n\tv_mul_f32 %1, %2, %1\n\tv_accvgpr_write_b32 a41, %1\n\t"
        "v_accvgpr_read_b32 %0, a42\n\tv_mul_f32 %0, %2, %0\n\tv_accvgpr_write_b32 a42, %0\n\t"
        "v_accvgpr_read_b32 %1, a43\n\tv_mul_f32 %1, %2, %1\n\tv_accvgpr_write_b32 a43, %1\n\t"
        "v_accvgpr_read_b32 %0, a44\n\tv_mul_f32 %0, %2, %0\n\tv_accvgpr_write_b32 a44, %0\n\t"
        "v_accvgpr_read_b32 %1, a45\n\tv_mul_f32 %1, %2, %1\n\tv_accvgpr_write_b32 a45, %1\n\t"
        "v_accvgpr_read_b32 %0, a46\n\tv_mul_f32 %0, %2, %0\n\tv_accvgpr_write_b32 a46, %0\n\t"
        "v_accvgpr_read_b32 %1, a47\n\tv_mul_f32 %1, %2, %1\n\tv_accvgpr_write_b32 a47, %1\n\t"
        "v_accvgpr_read_b32 %0, a48\n\tv_mul_f32 %0, %3, %0\n\tv_accvgpr_write_b32 a48, %0\n\t"
        "v_accvgpr_read_b32 %1, a49\n\tv_mul_f32 %1, %3, %1\n\tv_accvgpr_write_b32 a49, %1\n\t"
        "v_accvgpr_read_b32 %0, a50\n\tv_mul_f32 %0, %3, %0\n\tv_accvgpr_write_b32 a50, %0\n\t"
        "v_accvgpr_read_b32 %1, a51\n\tv_mul_f32 %1, %3, %1\n\tv_accvgpr_write_b32 a51, %1\n\t"
        "v_accvgpr_read_b32 %0, a52\n\tv_mul_f32 %0, %3, %0\n\tv_accvgpr_write_b32 a52, %0\n\t"
        "v_accvgpr_read_b32 %1, a53\n\tv_mul_f32 %1, %3, %1\n\tv_accvgpr_write_b32 a53, %1\n\t"
        "v_accvgpr_read_b32 %0, a54\n\tv_mul_f32 %0, %3, %0\n\tv_accvgpr_write_b32 a54, %0\n\t"
        "v_accvgpr_read_b32 %1, a55\n\tv_mul_f32 %1, %3, %1\n\tv_accvgpr_write_b32 a55, %1\n\t"
        "v_accvgpr_read_b32 %0, a56\n\tv_mul_f32 %0, %3, %0\n\tv_accvgpr_write_b32 a56, %0\n\t"
        "v_accvgpr_read_b32 %1, a57\n\tv_mul_f32 %1, %3, %1\n\tv_accvgpr_write_b32 a57, %1\n\t"
        "v_accvgpr_read_b32 %0, a58\n\tv_mul_f32 %0, %3, %0\n\tv_accvgpr_write_b32 a58, %0\n\t"
        "v_accvgpr_read_b32 %1, a59\n\tv_mul_f32 %1, %3, %1\n\tv_accvgpr_write_b32 a59, %1\n\t"
        "v_accvgpr_read_b32 %0, a60\n\tv_mul_f32 %0, %3, %0\n\tv_accvgpr_write_b32 a60, %0\n\t"
        "v_accvgpr_read_b32 %1, a61\n\tv_mul_f32 %1, %3, %1\n\tv_accvgpr_write_b32 a61, %1\n\t"
        "v_accvgpr_read_b32 %0, a62\n\tv_mul_f32 %0, %3, %0\n\tv_accvgpr_write_b32 a62, %0\n\t"
        "v_accvgpr_read_b32 %1, a63\n\tv_mul_f32 %1, %3, %1\n\tv_accvgpr_write_b32 a63, %1\n\t"
        "v_accvgpr_read_b32 %0, a64\n\tv_mul_f32 %0, %2, %0\n\tv_accvgpr_write_b32 a64, %0\n\t"
        "v_accvgpr_read_b32 %1, a65\n\tv_mul_f32 %1, %2, %1\n\tv_accvgpr_write_b32 a65, %1\n\t"
        "v_accvgpr_read_b32 %0, a66\n\tv_mul_f32 %0, %2, %0\n\tv_accvgpr_write_b32 a66, %0\n\t"
        "v_accvgpr_read_b32 %1, a67\n\tv_mul_f32 %1, %2, %1\n\tv_accvgpr_write_b32 a67, %1\n\t"
        "v_accvgpr_read_b32 %0, a68\n\tv_mul_f32 %0, %2, %0\n\tv_accvgpr_write_b32 a68, %0\n\t"
        "v_accvgpr_read_b32 %1, a69\n\tv_mul_f32 %1, %2, %1\n\tv_accvgpr_write_b32 a69, %1\n\t"
        "v_accvgpr_read_b32 %0, a70\n\tv_mul_f32 %0, %2, %0\n\tv_accvgpr_write_b32 a70, %0\n\t"
        "v_accvgpr_read_b32 %1, a71\n\tv_mul_f32 %1, %2, %1\n\tv_accvgpr_write_b32 a71, %1\n\t"
        "v_accvgpr_read_b32 %0, a72\n\tv_mul_f32 %0, %2, %0\n\tv_accvgpr_write_b32 a72, %0\n\t"
        "v_accvgpr_read_b32 %1, a73\n\tv_mul_f32 %1, %2, %1\n\tv_accvgpr_write_b32 a73, %1\n\t"
        "v_accvgpr_read_b32 %0, a74\n\tv_mul_f32 %0, %2, %0\n\tv_accvgpr_write_b32 a74, %0\n\t"
        "v_accvgpr_read_b32 %1, a75\n\tv_mul_f32 %1, %2, %1\n\tv_accvgpr_write_b32 a75, %1\n\t"
        "v_accvgpr_read_b32 %0, a76\n\tv_mul_f32 %0, %2, %0\n\tv_accvgpr_write_b32 a76, %0\n\t"
        "v_accvgpr_read_b32 %1, a77\n\tv_mul_f32 %1, %2, %1\n\tv_accvgpr_write_b32 a77, %1\n\t"
        "v_accvgpr_read_b32 %0, a78\n\tv_mul_f32 %0, %2, %0\n\tv_accvgpr_write_b32 a78, %0\n\t"
        "v_accvgpr_read_b32 %1, a79\n\tv_mul_f32 %1, %2, %1\n\tv_accvgpr_write_b32 a79, %1\n\t"
        "v_accvgpr_read_b32 %0, a80\n\tv_mul_f32 %0, %3, %0\n\tv_accvgpr_write_b32 a80, %0\n\t"
        "v_accvgpr_read_b32 %1, a81\n\tv_mul_f32 %1, %3, %1\n\tv_accvgpr_write_b32 a81, %1\n\t"
        "v_accvgpr_read_b32 %0, a82\n\tv_mul_f32 %0, %3, %0\n\tv_accvgpr_write_b32 a82, %0\n\t"
        "v_accvgpr_read_b32 %1, a83\n\tv_mul_f32 %1, %3, %1\n\tv_accvgpr_write_b32 a83, %1\n\t"
        "v_accvgpr_read_b32 %0, a84\n\tv_mul_f32 %0, %3, %0\n\tv_accvgpr_write_b32 a84, %0\n\t"
        "v_accvgpr_read_b32 %1, a85\n\tv_mul_f32 %1, %3, %1\n\tv_accvgpr_write_b32 a85, %1\n\t"
        "v_accvgpr_read_b32 %0, a86\n\tv_mul_f32 %0, %3, %0\n\tv_accvgpr_write_b32 a86, %0\n\t"
        "v_accvgpr_read_b32 %1, a87\n\tv_mul_f32 %1, %3, %1\n\tv_accvgpr_write_b32 a87, %1\n\t"
        "v_accvgpr_read_b32 %0, a88\n\tv_mul_f32 %0, %3, %0\n\tv_accvgpr_write_b32 a88, %0\n\t"
        "v_accvgpr_read_b32 %1, a89\n\tv_mul_f32 %1, %3, %1\n\tv_accvgpr_write_b32 a89, %1\n\t"
        "v_accvgpr_read_b32 %0, a90\n\tv_mul_f32 %0, %3, %0\n\tv_accvgpr_write_b32 a90, %0\n\t"
        "v_accvgpr_read_b32 %1, a91\n\tv_mul_f32 %1, %3, %1\n\tv_accvgpr_write_b32 a91, %1\n\t"
        "v_accvgpr_read_b32 %0, a92\n\tv_mul_f32 %0, %3, %0\n\tv_accvgpr_write_b32 a92, %0\n\t"
        "v_accvgpr_read_b32 %1, a93\n\tv_mul_f32 %1, %3, %1\n\tv_accvgpr_write_b32 a93, %1\n\t"
        "v_accvgpr_read_b32 %0, a94\n\tv_mul_f32 %0, %3, %0\n\tv_accvgpr_write_b32 a94, %0\n\t"
        "v_accvgpr_read_b32 %1, a95\n\tv_mul_f32 %1, %3, %1\n\tv_accvgpr_write_b32 a95, %1\n\t"
        "v_accvgpr_read_b32 %0, a96\n\tv_mul_f32 %0, %2, %0\n\tv_accvgpr_write_b32 a96, %0\n\t"
        "v_accvgpr_read_b32 %1, a97\n\tv_mul_f32 %1, %2, %1\n\tv_accvgpr_write_b32 a97, %1\n\t"
        "v_accvgpr_read_b32 %0, a98\n\tv_mul_f32 %0, %2, %0\n\tv_accvgpr_write_b32 a98, %0\n\t"
        "v_accvgpr_read_b32 %1, a99\n\tv_mul_f32 %1, %2, %1\n\tv_accvgpr_write_b32 a99, %1\n\t"
        "v_accvgpr_read_b32 %0, a100\n\tv_mul_f32 %0, %2, %0\n\tv_accvgpr_write_b32 a100, %0\n\t"
        "v_accvgpr_read_b32 %1, a101\n\tv_mul_f32 %1, %2, %1\n\tv_accvgpr_write_b32 a101, %1\n\t"
        "v_accvgpr_read_b32 %0, a102\n\tv_mul_f32 %0, %2, %0\n\tv_accvgpr_write_b32 a102, %0\n\t"
        "v_accvgpr_read_b32 %1, a103\n\tv_mul_f32 %1, %2, %1\n\tv_accvgpr_write_b32 a103, %1\n\t"
        "v_accvgpr_read_b32 %0, a104\n\tv_mul_f32 %0, %2, %0\n\tv_accvgpr_write_b32 a104, %0\n\t"
        "v_accvgpr_read_b32 %1, a105\n\tv_mul_f32 %1, %2, %1\n\tv_accvgpr_write_b32 a105, %1\n\t"
        "v_accvgpr_read_b32 %0, a106\n\tv_mul_f32 %0, %2, %0\n\tv_accvgpr_write_b32 a106, %0\n\t"
        "v_accvgpr_read_b32 %1, a107\n\tv_mul_f32 %1, %2, %1\n\tv_accvgpr_write_b32 a107, %1\n\t"
        "v_accvgpr_read_b32 %0, a108\n\tv_mul_f32 %0, %2, %0\n\tv_accvgpr_write_b32 a108, %0\n\t"
        "v_accvgpr_read_b32 %1, a109\n\tv_mul_f32 %1, %2, %1\n\tv_accvgpr_write_b32 a109, %1\n\t"
        "v_accvgpr_read_b32 %0, a110\n\tv_mul_f32 %0, %2, %0\n\tv_accvgpr_write_b32 a110, %0\n\t"
        "v_accvgpr_read_b32 %1, a111\n\tv_mul_f32 %1, %2, %1\n\tv_accvgpr_write_b32 a111, %1\n\t"
        "v_accvgpr_read_b32 %0, a112\n\tv_mul_f32 %0, %3, %0\n\tv_accvgpr_write_b32 a112, %0\n\t"
        "v_accvgpr_read_b32 %1, a113\n\tv_mul_f32 %1, %3, %1\n\tv_accvgpr_write_b32 a113, %1\n\t"
        "v_accvgpr_read_b32 %0, a114\n\tv_mul_f32 %0, %3, %0\n\tv_accvgpr_write_b32 a114, %0\n\t"
        "v_accvgpr_read_b32 %1, a115\n\tv_mul_f32 %1, %3, %1\n\tv_accvgpr_write_b32 a115, %1\n\t"
        "v_accvgpr_read_b32 %0, a116\n\tv_mul_f32 %0, %3, %0\n\tv_accvgpr_write_b32 a116, %0\n\t"
        "v_accvgpr_read_b32 %1, a117\n\tv_mul_f32 %1, %3, %1\n\tv_accvgpr_write_b32 a117, %1\n\t"
        "v_accvgpr_read_b32 %0, a118\n\tv_mul_f32 %0, %3, %0\n\tv_accvgpr_write_b32 a118, %0\n\t"
        "v_accvgpr_read_b32 %1, a119\n\tv_mul_f32 %1, %3, %1\n\tv_accvgpr_write_b32 a119, %1\n\t"
        "v_accvgpr_read_b32 %0, a120\n\tv_mul_f32 %0, %3, %0\n\tv_accvgpr_write_b32 a120, %0\n\t"
        "v_accvgpr_read_b32 %1, a121\n\tv_mul_f32 %1, %3, %1\n\tv_accvgpr_write_b32 a121, %1\n\t"
        "v_accvgpr_read_b32 %0, a122\n\tv_mul_f32 %0, %3, %0\n\tv_accvgpr_write_b32 a122, %0\n\t"
        "v_accvgpr_read_b32 %1, a123\n\tv_mul_f32 %1, %3, %1\n\tv_accvgpr_write_b32 a123, %1\n\t"
        "v_accvgpr_read_b32 %0, a124\n\tv_mul_f32 %0, %3, %0\n\tv_accvgpr_write_b32 a124, %0\n\t"
        "v_accvgpr_read_b32 %1, a125\n\tv_mul_f32 %1, %3, %1\n\tv_accvgpr_write_b32 a125, %1\n\t"
        "v_accvgpr_read_b32 %0, a126\n\tv_mul_f32 %0, %3, %0\n\tv_accvgpr_write_b32 a126, %0\n\t"
        "v_accvgpr_read_b32 %1, a127\n\tv_mul_f32 %1, %3, %1\n\tv_accvgpr_write_b32 a127, %1\n\t"
        "s_nop 1" : "=&v"(t0), "=&v"(t1) : "v"(c.fct[0]), "v"(c.fct[1]) : W4_ACLOB);
      c.lsum[0] = fmul_s(c.lsum[0], c.fct[0]); c.lsum[1] = fmul_s(c.lsum[1], c.fct[1]); c.pend = false;
    }
  }
  asm volatile("s_waitcnt lgkmcnt(0)" ::: "memory");
  GAP();
}
template <int X, bool PEEL, bool ZERO1, int DK = 1, int DV = 1, bool KR = true> DI void phaseB(W4C& c, const int t) { phaseB_<X, PEEL, ZERO1, DK, DV, KR>(c, t, make_iseq<32>{}); }

template <int... Is> DI void q_reads(W4C& c, unsigned qoff, iseq<Is...>) { (q_read<Is>(c, qoff), ...); }
template <int... Is> DI void k_reads(W4C& c, unsigned slot, iseq<Is...>) { (k_read<Is>(c, slot), ...); }
template <int... Is> DI void tr_reads(W4C& c, unsigned vslot, iseq<Is...>) { (tr_read<Is>(c, vslot), ...); }
template <int X, int... Cs> DI void packs(W4C& c, iseq<Cs...>) { (pack1<X, ((Cs >> 3) & 1), 2 * (Cs >> 4) + ((Cs >> 2) & 1), (Cs & 3)>(c), ...); }
template <int X, int J> DI void rowsum1_padded(W4C& c) { rowsum1<X, J>(c); asm volatile("s_nop 1"); }
template <int X, int... Js> DI void rowsums(W4C& c, iseq<Js...>) { (rowsum1_padded<X, Js>(c), ...); }
template <bool Z, int... Ms> DI void pv_all(W4C& c, iseq<Ms...>) { (pv_mfma<Ms, Z>(c), ...); }
template <int M> DI void pv_drain1(W4C& c, const unsigned (&vqsrc)[2]) {
  pv_mfma<M, false>(c);
  if constexpr (M & 1) {
    constexpr int qp = M >> 1;
    asm volatile("s_mov_b32 m0, %0\n\ts_nop 0\n\tbuffer_load_dwordx4 %1, %2, %3 offen lds"
                 :: "s"(c.m0q + 1024u * qp), "v"(vqsrc[qp & 1]), "s"(c.srdQn), "s"(c.qsofn + (unsigned)qp * c.qstepn) : "m0", "memory");
  }
  GAP();
}
template <int... Ms> DI void pv_drain(W4C& c, const unsigned (&vqsrc)[2], iseq<Ms...>) { (pv_drain1<Ms>(c, vqsrc), ...); }

DI void dma1(unsigned m0, unsigned vo, __amdgpu_buffer_rsrc_t srd, unsigned so) {
  asm volatile("s_mov_b32 m0, %0\n\ts_nop 0\n\tbuffer_load_dwordx4 %1, %2, %3 offen lds" :: "s"(m0), "v"(vo), "s"(srd), "s"(so) : "m0", "memory");
}
#define TS(i)
struct QP {
  f32x16 acc[4][2];
  u32x4 x[2][2][8];
  u32x2 b[2][4][2];
  f32x4 m[2][4];
  u32x4 hf[2][2];
  float t[4];
  unsigned trb[4][2], mvb, m0w, sofw, vw[4];
  unsigned xvo, pf;
  const char* xp;
  __amdgpu_buffer_rsrc_t srdW;
};
template <int J, int PC> DI void qp_w1(QP& s) { dma1(s.m0w + (J % 3) * 32768u + PC * 1024u, s.vw[PC & 3], s.srdW, s.sofw + (unsigned)(J * 128 + PC * 4) * 8192u); }
template <int I> DI void qp_kv1(W4C& c) {
  constexpr int q = I & 3;
  if constexpr (I < 4) dma1(c.m0k + 1024u * q, c.vksrc[q & 1], c.srdK, c.sofk + KRS * 4u * q);
  else if constexpr (I < 8) dma1(c.m0v + 1024u * q, c.vvsrc, c.srdV, c.sofv + KRS * 8u * (q >> 1) + 128u * (q & 1));
  else dma1(c.m0k + 0x4000u + 1024u * q, c.vksrc[q & 1], c.srdK, KTILEB + c.sofk + KRS * 4u * q);
}
constexpr int qp_nx(int N) { return N < 24 ? 2 : 0; }
constexpr int qp_no(int N) { return N < 4 ? 2 : N < 8 ? 1 : N < 20 ? 2 : 0; }
constexpr int qp_sum(int A, int B) { int r = 0; for (int m = A; m < B; ++m) r += qp_nx(m) + qp_no(m); return r; }
constexpr int qp_vm(int N) {
  int v = N >= 7 ? qp_no(N - 7) + qp_sum(N - 6, N) : 63;
  const int w = N == 7 ? qp_sum(4, 7) : N == 15 ? qp_sum(12, 15) : N == 23 ? qp_sum(16, 23) + 1 : 63;
  return v < w ? v : w;
}
template <int N, int I> DI void qp_other(QP& s, W4C& c) {
  if constexpr (N < 4) qp_w1<1, 2 * N + I>(s);
  else if constexpr (N < 8) { if constexpr (I == 0) qp_w1<2, N - 4>(s); }
  else if constexpr (N < 12) { if constexpr (I == 0) qp_w1<3, N - 8>(s); else qp_w1<2, N - 4>(s); }
  else if constexpr (N < 16) { if constexpr (I == 0) qp_w1<3, N - 8>(s); else qp_kv1<N - 12>(c); }
  else if constexpr (N < 20) qp_kv1<4 + 2 * (N - 16) + I>(c);
}
template <int N, int C> DI void qp_read1(QP& s) {
  constexpr int J = N >> 3, ks = N & 7;
  #pragma unroll
  for (int t = 0; t < 2; ++t)
    asm volatile("ds_read_b64_tr_b16 %0, %1 offset:%2" : "=&v"(s.b[N & 1][C][t]) : "v"(s.trb[C][t] + (J % 3 == 2 ? 65536u : 0u)), "i"((J % 3 == 1 ? 32768 : 0) + ks * 4096) : "memory");
}
template <int N, int Jx> DI void qp_readm1(QP& s) {
  asm volatile("ds_read_b128 %0, %1 offset:%2" : "=&v"(s.m[N & 1][Jx]) : "v"(s.mvb), "i"(N * 64 + (Jx & 1) * 16 + (Jx >> 1) * 2048) : "memory");
}
template <int N, int E> DI void qp_moda(QP& s) {
  asm("v_lshlrev_b32_e32 %0, 16, %4\n\tv_and_b32_e32 %1, 0xffff0000, %4\n\tv_lshlrev_b32_e32 %2, 16, %5\n\tv_and_b32_e32 %3, 0xffff0000, %5\n\t"
      "v_fma_f32 %0, %0, %6, %8\n\tv_fma_f32 %1, %1, %7, %9"
      : "=&v"(s.t[0]), "=&v"(s.t[1]), "=&v"(s.t[2]), "=&v"(s.t[3])
      : "v"(s.x[(N >> 3) & 1][0][N & 7][E]), "v"(s.x[(N >> 3) & 1][1][N & 7][E]),
        "v"(s.m[N & 1][2 + (E >> 1)][2 * (E & 1)]), "v"(s.m[N & 1][2 + (E >> 1)][2 * (E & 1) + 1]), "v"(s.m[N & 1][E >> 1][2 * (E & 1)]), "v"(s.m[N & 1][E >> 1][2 * (E & 1) + 1]));
}
template <int N, int E> DI void qp_modb(QP& s) {
  asm("v_fma_f32 %2, %2, %6, %8\n\tv_fma_f32 %3, %3, %7, %9\n\tv_cvt_pk_bf16_f32 %0, %4, %5\n\tv_cvt_pk_bf16_f32 %1, %2, %3"
      : "=&v"(s.hf[N & 1][0][E]), "=&v"(s.hf[N & 1][1][E]), "+v"(s.t[2]), "+v"(s.t[3])
      : "v"(s.t[0]), "v"(s.t[1]),
        "v"(s.m[N & 1][2 + (E >> 1)][2 * (E & 1)]), "v"(s.m[N & 1][2 + (E >> 1)][2 * (E & 1) + 1]), "v"(s.m[N & 1][E >> 1][2 * (E & 1)]), "v"(s.m[N & 1][E >> 1][2 * (E & 1) + 1]));
}
template <int N, int E> DI void qp_mod1(QP& s) { qp_moda<N, E>(s); qp_modb<N, E>(s); }
template <int J, int K> DI void qp_x1(QP& s) {
  #pragma unroll
  for (int qb = 0; qb < 2; ++qb)
    asm volatile("global_load_dwordx4 %0, %1, %2 offset:%3" : "=&v"(s.x[J & 1][qb][K]) : "v"(s.xvo), "s"(s.xp + (qb * 32 + J * 8 + (K & 4)) * 1024), "i"((K & 3) * 1024) : "memory");
}
template <int... Ks> DI void qp_x0(QP& s, iseq<Ks...>) { (qp_x1<0, Ks>(s), ...); }
template <int... Ps> DI void qp_w0(QP& s, iseq<Ps...>) { (qp_w1<0, Ps>(s), ...); }
template <int N, int C> DI void qp_grp(QP& s, W4C& c) {
  if constexpr (N > 0)
    asm volatile("s_waitcnt lgkmcnt(%6)" : "+v"(s.b[N & 1][C][0]), "+v"(s.b[N & 1][C][1]), "+v"(s.m[(N + 1) & 1][0]), "+v"(s.m[(N + 1) & 1][1]), "+v"(s.m[(N + 1) & 1][2]), "+v"(s.m[(N + 1) & 1][3])
                 : "i"(C == 0 ? 6 : N == 31 ? 6 - 2 * C : N == 30 ? 6 : 10) : "memory");
  if constexpr (N + 1 < 32) qp_read1<N + 1, C>(s);
  if constexpr (N + 2 < 32 && C == 0) { qp_readm1<N + 2, 0>(s); qp_readm1<N + 2, 1>(s); qp_readm1<N + 2, 2>(s); qp_readm1<N + 2, 3>(s); }
  const bf16x8 wf = __builtin_bit_cast(bf16x8, (u32x4){s.b[N & 1][C][0][0], s.b[N & 1][C][0][1], s.b[N & 1][C][1][0], s.b[N & 1][C][1][1]});
  s.acc[C][0] = __builtin_amdgcn_mfma_f32_32x32x16_bf16(wf, __builtin_bit_cast(bf16x8, s.hf[N & 1][0]), s.acc[C][0], 0, 0, 0);
  GAP();
  if constexpr (N + 1 < 32) qp_moda<N + 1, C>(s);
  GAP();
  s.acc[C][1] = __builtin_amdgcn_mfma_f32_32x32x16_bf16(wf, __builtin_bit_cast(bf16x8, s.hf[N & 1][1]), s.acc[C][1], 0, 0, 0);
  GAP();
  if constexpr (N + 1 < 32) qp_modb<N + 1, C>(s);
  if constexpr (C == 0 && N < 24) qp_x1<(N >> 3) + 1, N & 7>(s);
  if constexpr (C == 1) qp_other<N, 0>(s, c);
  if constexpr (C == 2) qp_other<N, 1>(s, c);
  GAP();
}
#define QP_WAIT(P, Q) asm volatile("s_waitcnt lgkmcnt(0)" : "+v"(s.b[P][0][0]), "+v"(s.b[P][0][1]), "+v"(s.b[P][1][0]), "+v"(s.b[P][1][1]), "+v"(s.b[P][2][0]), "+v"(s.b[P][2][1]), \
                 "+v"(s.b[P][3][0]), "+v"(s.b[P][3][1]), "+v"(s.m[Q][0]), "+v"(s.m[Q][1]), "+v"(s.m[Q][2]), "+v"(s.m[Q][3]) :: "memory")
template <int N> DI void qp_step(QP& s, W4C& c) {
  constexpr int M = N + 1 < 32 ? N + 1 : 31;
  if constexpr ((N & 7) == 7 && N < 31)
    asm volatile("s_waitcnt vmcnt(%2) lgkmcnt(0)\n\ts_barrier" : "+v"(s.x[(M >> 3) & 1][0][M & 7]), "+v"(s.x[(M >> 3) & 1][1][M & 7]) : "i"(qp_vm(N)), "v"(s.pf) : "memory");
  else if constexpr (N > 7 && N < 31)
    asm volatile("s_waitcnt vmcnt(%2)" : "+v"(s.x[(M >> 3) & 1][0][M & 7]), "+v"(s.x[(M >> 3) & 1][1][M & 7]) : "i"(qp_vm(N)) : "memory");
  if constexpr ((N & 7) == 7 && N < 31) TS(1 + (N >> 3));
  GAP();
  qp_grp<N, 0>(s, c); qp_grp<N, 1>(s, c); qp_grp<N, 2>(s, c); qp_grp<N, 3>(s, c);
}
template <int... Is> DI void qp_run(QP& s, W4C& c, iseq<Is...>) { (qp_step<Is>(s, c), ...); }
#define QP_TIE(B) asm volatile("" : "+v"(s.x[B][0][0]), "+v"(s.x[B][0][1]), "+v"(s.x[B][0][2]), "+v"(s.x[B][0][3]), "+v"(s.x[B][0][4]), "+v"(s.x[B][0][5]), "+v"(s.x[B][0][6]), "+v"(s.x[B][0][7]), \
                 "+v"(s.x[B][1][0]), "+v"(s.x[B][1][1]), "+v"(s.x[B][1][2]), "+v"(s.x[B][1][3]), "+v"(s.x[B][1][4]), "+v"(s.x[B][1][5]), "+v"(s.x[B][1][6]), "+v"(s.x[B][1][7]) :: "memory")

__global__ __launch_bounds__(256, 1)
void k12_attn(const u16* __restrict__ xh, const u16* __restrict__ wq, const float* __restrict__ mod,
              const u16* __restrict__ Kb, const u16* __restrict__ Vb, u16* __restrict__ Of,
              const u16* __restrict__ wo, const u16* __restrict__ wg, const u16* __restrict__ wu, const u16* __restrict__ wd,
              u16* __restrict__ wof, u16* __restrict__ wdf, u16* __restrict__ wguf) {
  extern __shared__ __attribute__((aligned(16))) char lds[];
  const unsigned tid = threadIdx.x, lane = tid & 63u, wid = rfl(tid >> 6), l32 = lane & 31u, hi = lane >> 5;
  constexpr int T = NT;
  const float C = 0.12751743082f;
  W4C c;
  c.T = T; c.THR = THR;
  c.ldsb = (unsigned)(size_t)lds;
  #pragma unroll
  for (int j = 0; j < 4; ++j) c.vkb[j] = c.ldsb + KRING + 256u * l32 + 16u * ((2u * j + hi) ^ (l32 & 7u));
  c.vvb = c.ldsb + VRING + (((lane & 3u) << 3) | (((lane >> 2) & 3u) << 6) | (((lane >> 4) & 1u) << 5) | (hi << 8));
  #pragma unroll
  for (int par = 0; par < 2; ++par) c.vksrc[par] = KRS * (lane >> 4) + 16u * ((lane & 15u) ^ (4u * par + (lane >> 4)));
  c.vvsrc = KRS * ((lane >> 2) & 7u) + 64u * hi + 16u * (lane & 3u);
  c.m0k = rfl(c.ldsb + KRING + 4096u * wid); c.m0v = rfl(c.ldsb + VRING + 4096u * wid);
  c.sofk = rfl(KRS * 16u * wid); c.sofv = rfl(KRS * 16u * wid);
  const unsigned bid = blockIdx.x, kvh = bid & 7u, head = kvh * 4u + ((bid >> 3) & 3u), y = bid >> 5;
  const unsigned rowblk = y * 8u + wid * 2u;
  c.srdK = __builtin_amdgcn_make_buffer_rsrc((void*)((const char*)Kb + (size_t)kvh * SK * 256), (short)0, SK * 256, SRD_WORD3);
  c.srdV = __builtin_amdgcn_make_buffer_rsrc((void*)((const char*)Vb + (size_t)kvh * SK * 256), (short)0, SK * 256, SRD_WORD3);
  const unsigned m0w = rfl(c.ldsb + WSTG + wid * 8192u);
  {
    QP s;
    s.xp = (const char*)(xh + (size_t)rowblk * 32u * 512u); s.xvo = lane * 16u;
    qp_x0(s, make_iseq<8>{});
    f32x4 mval;
    asm volatile("global_load_dwordx4 %0, %1, %2" : "=&v"(mval) : "v"(tid * 16u), "s"(mod) : "memory");
    s.srdW = __builtin_amdgcn_make_buffer_rsrc((void*)wq, (short)0, DM * HQ * DH * 2, SRD_WORD3);
    #pragma unroll
    for (int i = 0; i < 4; ++i) s.vw[i] = (lane >> 4) * 8192u + 16u * ((lane & 15u) ^ (((lane >> 4) << 2) | (unsigned)i));
    s.m0w = m0w; s.sofw = rfl(wid * 32u * 8192u + head * 256u);
    s.mvb = c.ldsb + VRING + 0x4000u + hi * 32u;
    {
      const unsigned blk = (lane >> 4) & 1u, q = (lane & 15u) >> 2, p = lane & 3u, pg = ((p & 1u) << 1) | (p >> 1);
      #pragma unroll
      for (int cc = 0; cc < 4; ++cc)
        #pragma unroll
        for (int t = 0; t < 2; ++t)
          s.trb[cc][t] = c.ldsb + WSTG + 256u * (8u * hi + 4u * t + q) + 16u * ((4u * cc + 2u * blk + (pg >> 1)) ^ ((q << 2) | ((2u * hi + t) & 3u))) + 8u * (pg & 1u);
    }
    qp_w0(s, make_iseq<8>{});
    s.pf = 0u;
    asm volatile("s_waitcnt vmcnt(0)" : "+v"(mval) :: "memory");
    TS(0);
    asm volatile("ds_write_b128 %0, %1" :: "v"(c.ldsb + VRING + 0x4000u + tid * 16u), "v"(mval) : "memory");
    #pragma unroll
    for (int i = 0; i < 4; ++i) { s.acc[i][0] = f32x16{}; s.acc[i][1] = f32x16{}; }
    asm volatile("s_waitcnt lgkmcnt(0)\n\ts_barrier" ::: "memory");
    QP_TIE(0);
    qp_readm1<0, 0>(s); qp_readm1<0, 1>(s); qp_readm1<0, 2>(s); qp_readm1<0, 3>(s);
    qp_readm1<1, 0>(s); qp_readm1<1, 1>(s); qp_readm1<1, 2>(s); qp_readm1<1, 3>(s);
    qp_read1<0, 0>(s); qp_read1<0, 1>(s); qp_read1<0, 2>(s); qp_read1<0, 3>(s);
    QP_WAIT(0, 0); QP_WAIT(0, 1);
    qp_mod1<0, 0>(s); qp_mod1<0, 1>(s); qp_mod1<0, 2>(s); qp_mod1<0, 3>(s);
    GAP();
    qp_run(s, c, make_iseq<32>{});
    TS(4);
    #pragma unroll
    for (int qb = 0; qb < 2; ++qb)
      #pragma unroll
      for (int d0 = 0; d0 < 8; ++d0) {
        u32x4 w;
        #pragma unroll
        for (int e = 0; e < 4; ++e) w[e] = cvtpk(s.acc[d0 >> 1][qb][8 * (d0 & 1) + 2 * e] * C, s.acc[d0 >> 1][qb][8 * (d0 & 1) + 2 * e + 1] * C);
        q_write(qb, d0, w);
      }
    TS(5);
    asm volatile("s_waitcnt vmcnt(0)" ::: "memory");
    TS(6);
  }
  asm volatile("s_waitcnt lgkmcnt(0)\n\ts_barrier" ::: "memory");
  k_reads(c, 0u, make_iseq<16>{});
  asm volatile("s_waitcnt lgkmcnt(0)\n\ts_nop 1" ::: "memory");
  c.mhat[0] = c.mhat[1] = 0.f; c.lsum[0] = c.lsum[1] = 0.f; c.fct[0] = c.fct[1] = 1.f; c.pend = false;
  {
    const unsigned u0 = rfl(bid * 4u + wid);
    const bool pk = u0 < 736u, two = u0 >= 736u && u0 < 896u;
    const unsigned pitch = pk ? 1024u : 3584u, r = lane >> 3, scc2 = pk ? 1024u : 65536u;
    #pragma unroll
    for (int U = 0; U < 2; ++U) {
      const unsigned u = U ? (two ? u0 + 288u : 1024u) : u0;
      const unsigned kind = u < 512u ? 0u : u < 736u ? 1u : u < 960u ? 2u : 3u;
      const unsigned v = u - (kind == 0u ? 0u : kind == 1u ? 512u : kind == 2u ? 736u : 960u);
      const unsigned kc64 = kind < 2u ? v >> 3 : v & 7u, ct = kind < 2u ? v & 7u : v >> 3;
      const bool on = U == 0 || two;
      c.srdR[U] = __builtin_amdgcn_make_buffer_rsrc((void*)(kind == 0u ? wo : kind == 1u ? wd : kind == 2u ? wg : wu), (short)0, on ? (kind == 0u ? HQ * DH : DF) * 1024 : 0, SRD_WORD3);
      c.rso[U] = rfl(on ? kc64 * 64u * pitch + ct * 128u : 0u); c.rsf[U] = c.rso[U];
      c.sdst[U] = kind == 0u ? wof + (size_t)(ct * 64u + kc64) * 4096u : kind == 1u ? wdf + (size_t)(ct * 28u + kc64) * 4096u
                             : wguf + (size_t)(ct * 65536u + kc64 * 4096u + (kind - 2u) * 512u);
      c.rn[U] = rfl(on ? 1u << 20 : 0u); c.rnr[U] = 0u;
      c.rm0[U] = rfl(c.ldsb + WSTG + wid * 18432u + U * 9216u);
    }
    c.rvo = (pk ? r : (r & 3u) + 8u * (r >> 2)) * pitch + 16u * ((lane & 7u) ^ (4u * ((lane >> 4) & 1u)));
    c.m0lim = c.rm0[0] + 8192u;
    c.rrel = 0u; c.rinc = rfl((pk ? 8u : 4u) * pitch); c.rtog = rfl(pk ? 0u : (4u * pitch) ^ (12u * pitch)); c.rlast = rfl((pk ? 56u : 52u) * pitch);
    c.sso = rfl(scc2 - 4096u); c.sinc = rfl(2048u - scc2); c.stog = rfl((2048u - scc2) ^ scc2);
    c.rit = 0u - 3u; c.radd = 2048u; c.rl16 = lane * 16u;
    const unsigned blk = (lane >> 4) & 1u, q = (lane & 15u) >> 2, p = lane & 3u;
    c.ra = c.rm0[0] - 4096u + 128u * (q + 4u * hi) + 16u * ((4u + 2u * blk + (p >> 1)) ^ (4u * (q >> 1))) + 8u * (p & 1u);
  }
  phaseA<0, true>(c, 0);  phaseB<0, true,  false>(c, 0);
  phaseA<1, false>(c, 1); phaseB<1, false, true>(c, 1);
  for (int t = 2; t < T - 2; t += 2) {
    phaseA<0, false>(c, t);     phaseB<0, false, false>(c, t);
    phaseA<1, false>(c, t + 1); phaseB<1, false, false>(c, t + 1);
  }
  phaseA<0, false>(c, T - 2); phaseB<0, false, false, 0, 1, true>(c, T - 2);
  phaseA<1, false, 0>(c, T - 1); phaseB<1, false, false, 0, 0, false>(c, T - 1);
  {
    const unsigned vslot = (unsigned)((T - 1) & 1) * 0x4000u;
    tr_reads(c, vslot, make_iseq<32>{});
    #pragma unroll
    for (int e = 0; e < 32; ++e) c.S[1][1][e >> 4][e & 15] = __builtin_amdgcn_exp2f(c.S[1][1][e >> 4][e & 15]);
    packs<1>(c, make_iseq<32>{});
    rowsums<1>(c, iseq<8, 9, 10, 11, 12, 13, 14, 15, 24, 25, 26, 27, 28, 29, 30, 31>{});
    #pragma unroll
    for (int qb = 0; qb < 2; ++qb) { const float rs = fadd_s(c.rs0[qb], c.rs1[qb]);
      auto pr = __builtin_amdgcn_permlane32_swap(__float_as_uint(rs), __float_as_uint(rs), false, false);
      c.lsum[qb] = fadd_s(c.lsum[qb], fadd_s(__uint_as_float(pr[0]), __uint_as_float(pr[1]))); }
    asm volatile("s_waitcnt lgkmcnt(0)" : W4_VLO :: "memory"); asm volatile("" : W4_VUP :: "memory");
    GAP();
    #pragma unroll
    for (int ks = 0; ks < 4; ++ks)
      #pragma unroll
      for (int db = 0; db < 4; ++db) c.Vf[db][ks] = __builtin_shufflevector(c.vlo[db][ks], c.vup[db][ks], 0, 1, 2, 3, 4, 5, 6, 7);
    asm volatile("s_nop 1" ::: "memory");
    pv_all<false>(c, make_iseq<32>{});
    asm volatile("s_nop 7\n\ts_nop 7" ::: "memory");
  }
  #pragma unroll
  for (int qb = 0; qb < 2; ++qb) {
    const float il = 1.f / c.lsum[qb];
    u16* op = Of + (((size_t)(rowblk + qb) * 256u + head * 8u) * 64u + lane) * 8u;
    #pragma unroll
    for (int db = 0; db < 4; ++db) {
      float o[16];
      W4_READ_O(db, qb, o);
      #pragma unroll
      for (int s = 0; s < 2; ++s) {
        u32x4 w = {cvtpk(o[8 * s + 0] * il, o[8 * s + 1] * il), cvtpk(o[8 * s + 2] * il, o[8 * s + 3] * il),
                   cvtpk(o[8 * s + 4] * il, o[8 * s + 5] * il), cvtpk(o[8 * s + 6] * il, o[8 * s + 7] * il)};
        st16(op + (2 * db + s) * 512, w);
      }
    }
  }
}

extern "C" void kernel_launch(void* const* d_in, const int* in_sizes, int n_in, void* d_out, int out_size, void* d_ws, size_t ws_size, hipStream_t stream) {
  const u16* x = (const u16*)d_in[0]; const u16* kc = (const u16*)d_in[1]; const u16* vc = (const u16*)d_in[2];
  const u16* wq = (const u16*)d_in[3]; const u16* wo = (const u16*)d_in[4];
  const u16* wg = (const u16*)d_in[6]; const u16* wu = (const u16*)d_in[7]; const u16* wd = (const u16*)d_in[8];
  const u16* cv = (const u16*)d_in[9]; const u16* wm = (const u16*)d_in[10];
  char* ws = (char*)d_ws;
  float* mod = (float*)(ws + WS_MOD); u16* xh = (u16*)(ws + WS_XH); u16* of = (u16*)(ws + WS_OF); u16* x1 = (u16*)(ws + WS_X1); u16* act = (u16*)(ws + WS_ACT); float* st = (float*)(ws + WS_ST); u16* wof = (u16*)(ws + WS_WO); u16* wdf = (u16*)(ws + WS_WD); u16* wguf = (u16*)(ws + WS_WG);
  (void)hipFuncSetAttribute((const void*)k12_attn, hipFuncAttributeMaxDynamicSharedMemorySize, (int)SHM_ATTN);
  (void)hipFuncSetAttribute((const void*)k_gemm64<4096, 0, true>, hipFuncAttributeMaxDynamicSharedMemorySize, 65536);
  (void)hipFuncSetAttribute((const void*)k_gemm64<1792, 1, false>, hipFuncAttributeMaxDynamicSharedMemorySize, 65536);
  (void)hipFuncSetAttribute((const void*)k5_gateup, hipFuncAttributeMaxDynamicSharedMemorySize, 135168);
  k0_prep<<<192 + SQ / 4, 256, 0, stream>>>(x, cv, wm, mod, xh);
  k12_attn<<<256, 256, SHM_ATTN, stream>>>(xh, wq, mod, kc, vc, of, wo, wg, wu, wd, wof, wdf, wguf);
  k_gemm64<4096, 0, true><<<256, 256, 65536, stream>>>(of, wof, mod, x, x1, nullptr, st);
  k5_gateup<<<224, 256, 135168, stream>>>(x1, wguf, mod, st, act);
  k_gemm64<1792, 1, false><<<256, 256, 65536, stream>>>(act, wdf, mod, nullptr, x1, (u16*)d_out, nullptr);
}
```
